```python
import torch

def _softmax(x, axis=-1):
    return torch.softmax(x, dim=axis)

def _gelu(x):
    return torch.nn.functional.gelu(x, approximate='tanh')

def _silu(x):
    return torch.nn.functional.silu(x)

def _sigmoid(x):
    return torch.sigmoid(x)

def _layernorm(x, axis=-1, eps=1e-05):
    m = x.mean(dim=axis, keepdim=True)
    v = x.var(dim=axis, keepdim=True, unbiased=False)
    return (x - m) / torch.sqrt(v + eps)

def _scatter_add(tgt, idx, src, axis=0):
    return tgt.clone().index_add_(axis, idx, src)

def _segment_sum(src, seg, n):
    out = torch.zeros((n,) + src.shape[1:], dtype=src.dtype)
    return out.index_add_(0, seg, src)

def _pad(x, pw):
    flat = tuple((v for pair in reversed(pw) for v in pair))
    return torch.nn.functional.pad(x, flat)
kernel_name = 'standalone__pixart__diffusion'

def setup_inputs():

    _w = lambda a: (lambda t: t.to(torch.bfloat16) if t.is_floating_point() else t)(torch.as_tensor(a))
    B, N, D, M = (2, 1024, 1152, 77)
    return {'x': _w((lambda *s: torch.randn(*s, dtype=torch.bfloat16) * 0.05)(B, N, D)), 'c': _w((lambda *s: torch.randn(*s, dtype=torch.bfloat16) * 0.05)(B, D)), 'y': _w((lambda *s: torch.randn(*s, dtype=torch.bfloat16) * 0.1)(B, M, D)), 'wqkv': _w((lambda *s: torch.randn(*s, dtype=torch.bfloat16) * 0.05)(D, 3 * D)), 'wo': _w((lambda *s: torch.randn(*s, dtype=torch.bfloat16) * 0.05)(D, D)), 'wq_c': _w((lambda *s: torch.randn(*s, dtype=torch.bfloat16) * 0.1)(D, D)), 'wkv_c': _w((lambda *s: torch.randn(*s, dtype=torch.bfloat16) * 0.1)(D, 2 * D)), 'wo_c': _w((lambda *s: torch.randn(*s, dtype=torch.bfloat16) * 0.05)(D, D)), 'w_mod': _w((lambda *s: torch.randn(*s, dtype=torch.bfloat16) * 0.05)(D, 6 * D))}

def reference(x, c, y, wqkv, wo, wq_c, wkv_c, wo_c, w_mod):
    B, N, D = x.shape
    mod = (_silu(c) @ w_mod).reshape(B, 6, 1, D)
    shift_a, scale_a, gate_a, shift_m, scale_m, gate_m = [mod[:, i] for i in range(6)]
    h = _layernorm(x, axis=-1) * (1 + scale_a) + shift_a
    qkv = h @ wqkv
    q, k, v = (qkv[..., :D].clone(), qkv[..., D:2 * D].clone(), qkv[..., 2 * D:].clone())
    p = _softmax(torch.einsum('bqd,bkd->bqk', q, k) * D ** (-0.5), axis=-1)
    x = x + gate_a * (torch.einsum('bqk,bkd->bqd', p, v) @ wo)
    hc = _layernorm(x, axis=-1)
    qc = hc @ wq_c
    kvc = y @ wkv_c
    kc, vc = (kvc[..., :D].clone(), kvc[..., D:].clone())
    pc = _softmax(torch.einsum('bqd,bkd->bqk', qc, kc) * D ** (-0.5), axis=-1)
    return x + torch.einsum('bqk,bkd->bqd', pc, vc) @ wo_c
```

```cpp
#include <hip/hip_runtime.h>
#include <hip/hip_bf16.h>
typedef unsigned short u16;
typedef __attribute__((ext_vector_type(8))) short bf16x8;
typedef __attribute__((ext_vector_type(4))) float f32x4;
constexpr int B_=2,N_=1024,D_=1152,M_=77;
__device__ __forceinline__ float bf2f(u16 v){return __uint_as_float(((unsigned)v)<<16);}
__device__ __forceinline__ u16 f2bf(float f){unsigned u=__float_as_uint(f);return (u16)((u+0x7fff+((u>>16)&1))>>16);}

__global__ void gemm_g(const u16*A,long sam,long sak,long bsA,const u16*B,long sbk,long sbn,long bsB,void*C,long scm,long bsC,int M,int N,int K,int mode,float scale){
  int lane=threadIdx.x&63, w=threadIdx.x>>6;
  int m0=blockIdx.y*64+ w*16, n0=blockIdx.x*16, b=blockIdx.z;
  A+=b*bsA;B+=b*bsB;
  f32x4 acc={0,0,0,0};
  int r=lane&15,q=lane>>4;
  for(int k0=0;k0<K;k0+=32){
    bf16x8 a,bb;
    for(int j=0;j<8;j++){int k=k0+q*8+j; int m=m0+r, n=n0+r;
      a[j]=(m<M&&k<K)?A[m*sam+k*sak]:0; bb[j]=(n<N&&k<K)?B[k*sbk+n*sbn]:0;}
    acc=__builtin_amdgcn_mfma_f32_16x16x32_bf16(a,bb,acc,0,0,0);
  }
  for(int j=0;j<4;j++){int m=m0+q*4+j,n=n0+r; if(m<M&&n<N){ if(mode==0)((u16*)C)[b*bsC+m*scm+n]=f2bf(acc[j]*scale); else ((float*)C)[b*bsC+m*scm+n]=acc[j]*scale;}}
}
__global__ void mod_k(const u16*c,const u16*wm,float*mod){
  int n=blockIdx.x*256+threadIdx.x; if(n>=3*D_)return;
  float a0=0,a1=0;
  for(int k=0;k<D_;k++){float w=bf2f(wm[(long)k*6*D_+n]); float c0=bf2f(c[k]),c1=bf2f(c[D_+k]);
    float s0=bf2f(f2bf(c0/(1.f+__expf(-c0)))),s1=bf2f(f2bf(c1/(1.f+__expf(-c1)))); a0+=s0*w;a1+=s1*w;}
  mod[n]=bf2f(f2bf(a0));mod[3*D_+n]=bf2f(f2bf(a1));
}
__device__ float wsum(float v){for(int o=32;o;o>>=1)v+=__shfl_xor(v,o);return v;}
__device__ float wmax(float v){for(int o=32;o;o>>=1)v=fmaxf(v,__shfl_xor(v,o));return v;}
__global__ void ln_k(const u16*x,const float*mod,u16*h,int domod){
  int row=blockIdx.x*4+(threadIdx.x>>6),lane=threadIdx.x&63; int b=row/N_;
  float v[18];float s=0;for(int i=0;i<18;i++){v[i]=bf2f(x[(long)row*D_+i*64+lane]);s+=v[i];}
  float m=wsum(s)/D_;float q=0;for(int i=0;i<18;i++){float d=v[i]-m;q+=d*d;}
  float r=rsqrtf(wsum(q)/D_+1e-5f);
  for(int i=0;i<18;i++){int d=i*64+lane;float y=(v[i]-m)*r; if(domod)y=y*(1.f+mod[b*3*D_+D_+d])+mod[b*3*D_+d]; h[(long)row*D_+d]=f2bf(y);}
}
__global__ void sm_k(const float*S,u16*P,int n,int ld,int ldp){
  int row=blockIdx.x*4+(threadIdx.x>>6),lane=threadIdx.x&63;
  float m=-1e30f;for(int i=lane;i<n;i+=64)m=fmaxf(m,S[(long)row*ld+i]);m=wmax(m);
  float s=0;for(int i=lane;i<n;i+=64)s+=__expf(S[(long)row*ld+i]-m);s=wsum(s);
  for(int i=lane;i<n;i+=64)P[(long)row*ldp+i]=f2bf(__expf(S[(long)row*ld+i]-m)/s);
}
__global__ void res_k(const u16*x,const float*o,const float*mod,u16*y,int gate){
  long i=(long)blockIdx.x*256+threadIdx.x; int b=i/((long)N_*D_),d=i%D_;
  float g=gate?mod[b*3*D_+2*D_+d]:1.f; y[i]=f2bf(bf2f(x[i])+g*o[i]);
}
extern "C" void kernel_launch(void* const* d_in,const int* in_sizes,int n_in,void* d_out,int out_size,void* d_ws,size_t ws_size,hipStream_t stream){
  const u16*x=(const u16*)d_in[0],*c=(const u16*)d_in[1],*y=(const u16*)d_in[2],*wqkv=(const u16*)d_in[3],*wo=(const u16*)d_in[4],*wqc=(const u16*)d_in[5],*wkvc=(const u16*)d_in[6],*woc=(const u16*)d_in[7],*wm=(const u16*)d_in[8];
  char*ws=(char*)d_ws; size_t o=0; auto al=[&](size_t b){void*p=ws+o;o+=(b+255)/256*256;return p;};
  float*mod=(float*)al(2*3*D_*4); u16*h=(u16*)al(2048L*D_*2); u16*Wqk=(u16*)al((long)D_*D_*2),*Wvo=(u16*)al((long)D_*D_*2);
  u16*T=(u16*)al(2048L*D_*2),*V=(u16*)al(2048L*D_*2); float*S=(float*)al(2048L*1024*4); u16*P=(u16*)al(2048L*1024*2);
  float*O=(float*)al(2048L*D_*4); u16*x1=(u16*)al(2048L*D_*2),*hc=(u16*)al(2048L*D_*2); u16*kvc=(u16*)al(154L*2*D_*2);
  u16*Wqc=(u16*)al(2L*D_*80*2),*Voc=(u16*)al(2L*80*D_*2); float*Sc=(float*)al(2048L*80*4); u16*Pc=(u16*)al(2048L*80*2);
  float sc=1.f/sqrtf((float)D_);
  auto G=[&](const u16*A,long sam,long sak,long bsA,const u16*Bm,long sbk,long sbn,long bsB,void*C,long scm,long bsC,int M,int N,int K,int nb,int mode,float s){
    gemm_g<<<dim3((N+15)/16,(M+63)/64,nb),256,0,stream>>>(A,sam,sak,bsA,Bm,sbk,sbn,bsB,C,scm,bsC,M,N,K,mode,s);};
  mod_k<<<(3*D_+255)/256,256,0,stream>>>(c,wm,mod);
  ln_k<<<512,256,0,stream>>>(x,mod,h,1);
  G(wqkv,3*D_,1,0,wqkv+D_,1,3*D_,0,Wqk,D_,0,D_,D_,D_,1,0,1.f);
  G(wqkv+2*D_,3*D_,1,0,wo,D_,1,0,Wvo,D_,0,D_,D_,D_,1,0,1.f);
  G(h,D_,1,0,Wqk,D_,1,0,T,D_,0,2048,D_,D_,1,0,1.f);
  G(h,D_,1,0,Wvo,D_,1,0,V,D_,0,2048,D_,D_,1,0,1.f);
  G(T,D_,1,1024L*D_,h,1,D_,1024L*D_,S,1024,1024L*1024,1024,1024,D_,2,1,sc);
  sm_k<<<512,256,0,stream>>>(S,P,1024,1024,1024);
  G(P,1024,1,1024L*1024,V,D_,1,1024L*D_,O,D_,1024L*D_,1024,D_,1024,2,1,1.f);
  res_k<<<2048*D_/256,256,0,stream>>>(x,O,mod,x1,1);
  ln_k<<<512,256,0,stream>>>(x1,mod,hc,0);
  G(y,D_,1,0,wkvc,2*D_,1,0,kvc,2*D_,0,154,2*D_,D_,1,0,1.f);
  G(wqc,D_,1,0,kvc,1,2*D_,77L*2*D_,Wqc,80,(long)D_*80,D_,77,D_,2,0,1.f);
  G(kvc+D_,2*D_,1,77L*2*D_,woc,D_,1,0,Voc,D_,80L*D_,77,D_,D_,2,0,1.f);
  G(hc,D_,1,1024L*D_,Wqc,80,1,(long)D_*80,Sc,80,1024L*80,1024,77,D_,2,1,sc);
  sm_k<<<512,256,0,stream>>>(Sc,Pc,77,80,80);
  G(Pc,80,1,1024L*80,Voc,D_,1,80L*D_,O,D_,1024L*D_,1024,D_,77,2,1,1.f);
  res_k<<<2048*D_/256,256,0,stream>>>(x1,O,mod,(u16*)d_out,0);
}
```

```cpp
#include <hip/hip_runtime.h>
#include <hip/hip_bf16.h>
typedef unsigned short u16;
typedef __attribute__((ext_vector_type(8))) short bf16x8;
typedef __attribute__((ext_vector_type(4))) float f32x4;
constexpr int B_=2,N_=1024,D_=1152,M_=77;
__device__ __forceinline__ float bf2f(u16 v){return __uint_as_float(((unsigned)v)<<16);}
__device__ __forceinline__ u16 f2bf(float f){unsigned u=__float_as_uint(f);return (u16)((u+0x7fff+((u>>16)&1))>>16);}

__global__ void gemm_g(const u16*A,long sam,long sak,long bsA,const u16*B,long sbk,long sbn,long bsB,void*C,long scm,long bsC,int M,int N,int K,int mode,float scale){
  int lane=threadIdx.x&63, w=threadIdx.x>>6;
  int m0=blockIdx.y*64+ w*16, n0=blockIdx.x*16, b=blockIdx.z;
  A+=b*bsA;B+=b*bsB;
  f32x4 acc={0,0,0,0};
  int r=lane&15,q=lane>>4;
  for(int k0=0;k0<K;k0+=32){
    bf16x8 a,bb;
    for(int j=0;j<8;j++){int k=k0+q*8+j; int m=m0+r, n=n0+r;
      a[j]=(m<M&&k<K)?A[m*sam+k*sak]:0; bb[j]=(n<N&&k<K)?B[k*sbk+n*sbn]:0;}
    acc=__builtin_amdgcn_mfma_f32_16x16x32_bf16(a,bb,acc,0,0,0);
  }
  for(int j=0;j<4;j++){int m=m0+q*4+j,n=n0+r; if(m<M&&n<N){ if(mode==0)((u16*)C)[b*bsC+m*scm+n]=f2bf(acc[j]*scale); else ((float*)C)[b*bsC+m*scm+n]=acc[j]*scale;}}
}
__global__ void mod_k(const u16*c,const u16*wm,float*mod){
  int n=blockIdx.x*256+threadIdx.x; if(n>=3*D_)return;
  float a0=0,a1=0;
  for(int k=0;k<D_;k++){float w=bf2f(wm[(long)k*6*D_+n]); float c0=bf2f(c[k]),c1=bf2f(c[D_+k]);
    float s0=bf2f(f2bf(c0/(1.f+__expf(-c0)))),s1=bf2f(f2bf(c1/(1.f+__expf(-c1)))); a0+=s0*w;a1+=s1*w;}
  mod[n]=bf2f(f2bf(a0));mod[3*D_+n]=bf2f(f2bf(a1));
}
__device__ float wsum(float v){for(int o=32;o;o>>=1)v+=__shfl_xor(v,o);return v;}
__device__ float wmax(float v){for(int o=32;o;o>>=1)v=fmaxf(v,__shfl_xor(v,o));return v;}
__global__ void ln_k(const u16*x,const float*mod,u16*h,int domod){
  int row=blockIdx.x*4+(threadIdx.x>>6),lane=threadIdx.x&63; int b=row/N_;
  float v[18];float s=0;for(int i=0;i<18;i++){v[i]=bf2f(x[(long)row*D_+i*64+lane]);s+=v[i];}
  float m=wsum(s)/D_;float q=0;for(int i=0;i<18;i++){float d=v[i]-m;q+=d*d;}
  float r=rsqrtf(wsum(q)/D_+1e-5f);
  for(int i=0;i<18;i++){int d=i*64+lane;float y=(v[i]-m)*r; if(domod)y=y*(1.f+mod[b*3*D_+D_+d])+mod[b*3*D_+d]; h[(long)row*D_+d]=f2bf(y);}
}
__global__ void sm_k(const float*S,u16*P,int n,int ld,int ldp){
  int row=blockIdx.x*4+(threadIdx.x>>6),lane=threadIdx.x&63;
  float m=-1e30f;for(int i=lane;i<n;i+=64)m=fmaxf(m,S[(long)row*ld+i]);m=wmax(m);
  float s=0;for(int i=lane;i<n;i+=64)s+=__expf(S[(long)row*ld+i]-m);s=wsum(s);
  for(int i=lane;i<n;i+=64)P[(long)row*ldp+i]=f2bf(__expf(S[(long)row*ld+i]-m)/s);
}
__global__ void res_k(const u16*x,const float*o,const float*mod,u16*y,int gate){
  long i=(long)blockIdx.x*256+threadIdx.x; int b=i/((long)N_*D_),d=i%D_;
  float g=gate?mod[b*3*D_+2*D_+d]:1.f; y[i]=f2bf(bf2f(x[i])+g*o[i]);
}

typedef __attribute__((ext_vector_type(4))) int i32x4;
struct PA{const i32x4*p[8]; long n[8];};
__global__ __launch_bounds__(256) void probe_k(const i32x4*p0,long n0,const i32x4*p1,long n1,const i32x4*p2,long n2,const i32x4*p3,long n3,int*sink){
  unsigned long long t0=__builtin_amdgcn_s_memrealtime();
  i32x4 acc={0,0,0,0};
  const i32x4* ps[4]={p0,p1,p2,p3}; long ns[4]={n0,n1,n2,n3};
  #pragma unroll
  for(int a=0;a<4;a++){
    long per=ns[a]/256; const i32x4*q=ps[a]+blockIdx.x*per;
    #pragma unroll 8
    for(long i=threadIdx.x;i<per;i+=256) acc^=q[i];
  }
  if((acc.x^acc.y^acc.z^acc.w)==0x12345677) sink[0]=1;
  __syncthreads();
  unsigned long long t1=__builtin_amdgcn_s_memrealtime();
  unsigned long long dt=(t1-t0)*1000;
  while(__builtin_amdgcn_s_memrealtime()-t1<dt) __builtin_amdgcn_s_sleep(32);
}
extern "C" void kernel_launch(void* const* d_in,const int* in_sizes,int n_in,void* d_out,int out_size,void* d_ws,size_t ws_size,hipStream_t stream){
  const u16*x=(const u16*)d_in[0],*c=(const u16*)d_in[1],*y=(const u16*)d_in[2],*wqkv=(const u16*)d_in[3],*wo=(const u16*)d_in[4],*wqc=(const u16*)d_in[5],*wkvc=(const u16*)d_in[6],*woc=(const u16*)d_in[7],*wm=(const u16*)d_in[8];
  char*ws=(char*)d_ws; size_t o=0; auto al=[&](size_t b){void*p=ws+o;o+=(b+255)/256*256;return p;};
  float*mod=(float*)al(2*3*D_*4); u16*h=(u16*)al(2048L*D_*2); u16*Wqk=(u16*)al((long)D_*D_*2),*Wvo=(u16*)al((long)D_*D_*2);
  u16*T=(u16*)al(2048L*D_*2),*V=(u16*)al(2048L*D_*2); float*S=(float*)al(2048L*1024*4); u16*P=(u16*)al(2048L*1024*2);
  float*O=(float*)al(2048L*D_*4); u16*x1=(u16*)al(2048L*D_*2),*hc=(u16*)al(2048L*D_*2); u16*kvc=(u16*)al(154L*2*D_*2);
  u16*Wqc=(u16*)al(2L*D_*80*2),*Voc=(u16*)al(2L*80*D_*2); float*Sc=(float*)al(2048L*80*4); u16*Pc=(u16*)al(2048L*80*2);
  float sc=1.f/sqrtf((float)D_);
  auto G=[&](const u16*A,long sam,long sak,long bsA,const u16*Bm,long sbk,long sbn,long bsB,void*C,long scm,long bsC,int M,int N,int K,int nb,int mode,float s){
    gemm_g<<<dim3((N+15)/16,(M+63)/64,nb),256,0,stream>>>(A,sam,sak,bsA,Bm,sbk,sbn,bsB,C,scm,bsC,M,N,K,mode,s);};
  probe_k<<<256,256,0,stream>>>((const i32x4*)wqkv,(long)D_*3*D_/8,(const i32x4*)wo,(long)D_*D_/8,(const i32x4*)x,2048L*D_/8,(const i32x4*)wkvc,(long)D_*2*D_/8,(int*)(ws+ws_size-64));
  mod_k<<<(3*D_+255)/256,256,0,stream>>>(c,wm,mod);
  ln_k<<<512,256,0,stream>>>(x,mod,h,1);
  G(wqkv,3*D_,1,0,wqkv+D_,1,3*D_,0,Wqk,D_,0,D_,D_,D_,1,0,1.f);
  G(wqkv+2*D_,3*D_,1,0,wo,D_,1,0,Wvo,D_,0,D_,D_,D_,1,0,1.f);
  G(h,D_,1,0,Wqk,D_,1,0,T,D_,0,2048,D_,D_,1,0,1.f);
  G(h,D_,1,0,Wvo,D_,1,0,V,D_,0,2048,D_,D_,1,0,1.f);
  G(T,D_,1,1024L*D_,h,1,D_,1024L*D_,S,1024,1024L*1024,1024,1024,D_,2,1,sc);
  sm_k<<<512,256,0,stream>>>(S,P,1024,1024,1024);
  G(P,1024,1,1024L*1024,V,D_,1,1024L*D_,O,D_,1024L*D_,1024,D_,1024,2,1,1.f);
  res_k<<<2048*D_/256,256,0,stream>>>(x,O,mod,x1,1);
  ln_k<<<512,256,0,stream>>>(x1,mod,hc,0);
  G(y,D_,1,0,wkvc,2*D_,1,0,kvc,2*D_,0,154,2*D_,D_,1,0,1.f);
  G(wqc,D_,1,0,kvc,1,2*D_,77L*2*D_,Wqc,80,(long)D_*80,D_,77,D_,2,0,1.f);
  G(kvc+D_,2*D_,1,77L*2*D_,woc,D_,1,0,Voc,D_,80L*D_,77,D_,D_,2,0,1.f);
  G(hc,D_,1,1024L*D_,Wqc,80,1,(long)D_*80,Sc,80,1024L*80,1024,77,D_,2,1,sc);
  sm_k<<<512,256,0,stream>>>(Sc,Pc,77,80,80);
  G(Pc,80,1,1024L*80,Voc,D_,1,80L*D_,O,D_,1024L*D_,1024,D_,77,2,1,1.f);
  res_k<<<2048*D_/256,256,0,stream>>>(x1,O,mod,(u16*)d_out,0);
}
```

```cpp
#include <hip/hip_runtime.h>
#include <hip/hip_bf16.h>
typedef unsigned short u16;
typedef __attribute__((ext_vector_type(8))) short bf16x8;
typedef __attribute__((ext_vector_type(4))) float f32x4;
constexpr int B_=2,N_=1024,D_=1152,M_=77;
__device__ __forceinline__ float bf2f(u16 v){return __uint_as_float(((unsigned)v)<<16);}
__device__ __forceinline__ u16 f2bf(float f){unsigned u=__float_as_uint(f);return (u16)((u+0x7fff+((u>>16)&1))>>16);}

__global__ void gemm_g(const u16*A,long sam,long sak,long bsA,const u16*B,long sbk,long sbn,long bsB,void*C,long scm,long bsC,int M,int N,int K,int mode,float scale){
  int lane=threadIdx.x&63, w=threadIdx.x>>6;
  int m0=blockIdx.y*64+ w*16, n0=blockIdx.x*16, b=blockIdx.z;
  A+=b*bsA;B+=b*bsB;
  f32x4 acc={0,0,0,0};
  int r=lane&15,q=lane>>4;
  for(int k0=0;k0<K;k0+=32){
    bf16x8 a,bb;
    for(int j=0;j<8;j++){int k=k0+q*8+j; int m=m0+r, n=n0+r;
      a[j]=(m<M&&k<K)?A[m*sam+k*sak]:0; bb[j]=(n<N&&k<K)?B[k*sbk+n*sbn]:0;}
    acc=__builtin_amdgcn_mfma_f32_16x16x32_bf16(a,bb,acc,0,0,0);
  }
  for(int j=0;j<4;j++){int m=m0+q*4+j,n=n0+r; if(m<M&&n<N){ if(mode==0)((u16*)C)[b*bsC+m*scm+n]=f2bf(acc[j]*scale); else ((float*)C)[b*bsC+m*scm+n]=acc[j]*scale;}}
}
__global__ void mod_k(const u16*c,const u16*wm,float*mod){
  int n=blockIdx.x*256+threadIdx.x; if(n>=3*D_)return;
  float a0=0,a1=0;
  for(int k=0;k<D_;k++){float w=bf2f(wm[(long)k*6*D_+n]); float c0=bf2f(c[k]),c1=bf2f(c[D_+k]);
    float s0=bf2f(f2bf(c0/(1.f+__expf(-c0)))),s1=bf2f(f2bf(c1/(1.f+__expf(-c1)))); a0+=s0*w;a1+=s1*w;}
  mod[n]=bf2f(f2bf(a0));mod[3*D_+n]=bf2f(f2bf(a1));
}
__device__ float wsum(float v){for(int o=32;o;o>>=1)v+=__shfl_xor(v,o);return v;}
__device__ float wmax(float v){for(int o=32;o;o>>=1)v=fmaxf(v,__shfl_xor(v,o));return v;}
__global__ void ln_k(const u16*x,const float*mod,u16*h,int domod){
  int row=blockIdx.x*4+(threadIdx.x>>6),lane=threadIdx.x&63; int b=row/N_;
  float v[18];float s=0;for(int i=0;i<18;i++){v[i]=bf2f(x[(long)row*D_+i*64+lane]);s+=v[i];}
  float m=wsum(s)/D_;float q=0;for(int i=0;i<18;i++){float d=v[i]-m;q+=d*d;}
  float r=rsqrtf(wsum(q)/D_+1e-5f);
  for(int i=0;i<18;i++){int d=i*64+lane;float y=(v[i]-m)*r; if(domod)y=y*(1.f+mod[b*3*D_+D_+d])+mod[b*3*D_+d]; h[(long)row*D_+d]=f2bf(y);}
}
__global__ void sm_k(const float*S,u16*P,int n,int ld,int ldp){
  int row=blockIdx.x*4+(threadIdx.x>>6),lane=threadIdx.x&63;
  float m=-1e30f;for(int i=lane;i<n;i+=64)m=fmaxf(m,S[(long)row*ld+i]);m=wmax(m);
  float s=0;for(int i=lane;i<n;i+=64)s+=__expf(S[(long)row*ld+i]-m);s=wsum(s);
  for(int i=lane;i<n;i+=64)P[(long)row*ldp+i]=f2bf(__expf(S[(long)row*ld+i]-m)/s);
}
__global__ void res_k(const u16*x,const float*o,const float*mod,u16*y,int gate){
  long i=(long)blockIdx.x*256+threadIdx.x; int b=i/((long)N_*D_),d=i%D_;
  float g=gate?mod[b*3*D_+2*D_+d]:1.f; y[i]=f2bf(bf2f(x[i])+g*o[i]);
}
__global__ __launch_bounds__(256) void probe_k(int*sink){
  unsigned long long t0=__builtin_amdgcn_s_memrealtime();
  asm volatile("s_waitcnt lgkmcnt(0)");

  unsigned long long t1=__builtin_amdgcn_s_memrealtime();
  unsigned long long dt=(t1-t0)*1000;
  while(__builtin_amdgcn_s_memrealtime()-t1<dt) __builtin_amdgcn_s_sleep(32);
}
extern "C" void kernel_launch(void* const* d_in,const int* in_sizes,int n_in,void* d_out,int out_size,void* d_ws,size_t ws_size,hipStream_t stream){
  const u16*x=(const u16*)d_in[0],*c=(const u16*)d_in[1],*y=(const u16*)d_in[2],*wqkv=(const u16*)d_in[3],*wo=(const u16*)d_in[4],*wqc=(const u16*)d_in[5],*wkvc=(const u16*)d_in[6],*woc=(const u16*)d_in[7],*wm=(const u16*)d_in[8];
  char*ws=(char*)d_ws; size_t o=0; auto al=[&](size_t b){void*p=ws+o;o+=(b+255)/256*256;return p;};
  float*mod=(float*)al(2*3*D_*4); u16*h=(u16*)al(2048L*D_*2); u16*Wqk=(u16*)al((long)D_*D_*2),*Wvo=(u16*)al((long)D_*D_*2);
  u16*T=(u16*)al(2048L*D_*2),*V=(u16*)al(2048L*D_*2); float*S=(float*)al(2048L*1024*4); u16*P=(u16*)al(2048L*1024*2);
  float*O=(float*)al(2048L*D_*4); u16*x1=(u16*)al(2048L*D_*2),*hc=(u16*)al(2048L*D_*2); u16*kvc=(u16*)al(154L*2*D_*2);
  u16*Wqc=(u16*)al(2L*D_*80*2),*Voc=(u16*)al(2L*80*D_*2); float*Sc=(float*)al(2048L*80*4); u16*Pc=(u16*)al(2048L*80*2);
  float sc=1.f/sqrtf((float)D_);
  auto G=[&](const u16*A,long sam,long sak,long bsA,const u16*Bm,long sbk,long sbn,long bsB,void*C,long scm,long bsC,int M,int N,int K,int nb,int mode,float s){
    gemm_g<<<dim3((N+15)/16,(M+63)/64,nb),256,0,stream>>>(A,sam,sak,bsA,Bm,sbk,sbn,bsB,C,scm,bsC,M,N,K,mode,s);};
  probe_k<<<256,256,0,stream>>>((int*)(ws+ws_size-64));
  mod_k<<<(3*D_+255)/256,256,0,stream>>>(c,wm,mod);
  ln_k<<<512,256,0,stream>>>(x,mod,h,1);
  G(wqkv,3*D_,1,0,wqkv+D_,1,3*D_,0,Wqk,D_,0,D_,D_,D_,1,0,1.f);
  G(wqkv+2*D_,3*D_,1,0,wo,D_,1,0,Wvo,D_,0,D_,D_,D_,1,0,1.f);
  G(h,D_,1,0,Wqk,D_,1,0,T,D_,0,2048,D_,D_,1,0,1.f);
  G(h,D_,1,0,Wvo,D_,1,0,V,D_,0,2048,D_,D_,1,0,1.f);
  G(T,D_,1,1024L*D_,h,1,D_,1024L*D_,S,1024,1024L*1024,1024,1024,D_,2,1,sc);
  sm_k<<<512,256,0,stream>>>(S,P,1024,1024,1024);
  G(P,1024,1,1024L*1024,V,D_,1,1024L*D_,O,D_,1024L*D_,1024,D_,1024,2,1,1.f);
  res_k<<<2048*D_/256,256,0,stream>>>(x,O,mod,x1,1);
  ln_k<<<512,256,0,stream>>>(x1,mod,hc,0);
  G(y,D_,1,0,wkvc,2*D_,1,0,kvc,2*D_,0,154,2*D_,D_,1,0,1.f);
  G(wqc,D_,1,0,kvc,1,2*D_,77L*2*D_,Wqc,80,(long)D_*80,D_,77,D_,2,0,1.f);
  G(kvc+D_,2*D_,1,77L*2*D_,woc,D_,1,0,Voc,D_,80L*D_,77,D_,D_,2,0,1.f);
  G(hc,D_,1,1024L*D_,Wqc,80,1,(long)D_*80,Sc,80,1024L*80,1024,77,D_,2,1,sc);
  sm_k<<<512,256,0,stream>>>(Sc,Pc,77,80,80);
  G(Pc,80,1,1024L*80,Voc,D_,1,80L*D_,O,D_,1024L*D_,1024,D_,77,2,1,1.f);
  res_k<<<2048*D_/256,256,0,stream>>>(x1,O,mod,(u16*)d_out,0);
}
```

```cpp
#include <hip/hip_runtime.h>
#include <hip/hip_bf16.h>
typedef unsigned short u16;
typedef __attribute__((ext_vector_type(8))) short bf16x8;
typedef __attribute__((ext_vector_type(4))) float f32x4;
constexpr int B_=2,N_=1024,D_=1152,M_=77;
__device__ __forceinline__ float bf2f(u16 v){return __uint_as_float(((unsigned)v)<<16);}
__device__ __forceinline__ u16 f2bf(float f){unsigned u=__float_as_uint(f);return (u16)((u+0x7fff+((u>>16)&1))>>16);}

__global__ void gemm_g(const u16*A,long sam,long sak,long bsA,const u16*B,long sbk,long sbn,long bsB,void*C,long scm,long bsC,int M,int N,int K,int mode,float scale){
  int lane=threadIdx.x&63, w=threadIdx.x>>6;
  int m0=blockIdx.y*64+ w*16, n0=blockIdx.x*16, b=blockIdx.z;
  A+=b*bsA;B+=b*bsB;
  f32x4 acc={0,0,0,0};
  int r=lane&15,q=lane>>4;
  for(int k0=0;k0<K;k0+=32){
    bf16x8 a,bb;
    for(int j=0;j<8;j++){int k=k0+q*8+j; int m=m0+r, n=n0+r;
      a[j]=(m<M&&k<K)?A[m*sam+k*sak]:0; bb[j]=(n<N&&k<K)?B[k*sbk+n*sbn]:0;}
    acc=__builtin_amdgcn_mfma_f32_16x16x32_bf16(a,bb,acc,0,0,0);
  }
  for(int j=0;j<4;j++){int m=m0+q*4+j,n=n0+r; if(m<M&&n<N){ if(mode==0)((u16*)C)[b*bsC+m*scm+n]=f2bf(acc[j]*scale); else ((float*)C)[b*bsC+m*scm+n]=acc[j]*scale;}}
}
__global__ void mod_k(const u16*c,const u16*wm,float*mod){
  int n=blockIdx.x*256+threadIdx.x; if(n>=3*D_)return;
  float a0=0,a1=0;
  for(int k=0;k<D_;k++){float w=bf2f(wm[(long)k*6*D_+n]); float c0=bf2f(c[k]),c1=bf2f(c[D_+k]);
    float s0=bf2f(f2bf(c0/(1.f+__expf(-c0)))),s1=bf2f(f2bf(c1/(1.f+__expf(-c1)))); a0+=s0*w;a1+=s1*w;}
  mod[n]=bf2f(f2bf(a0));mod[3*D_+n]=bf2f(f2bf(a1));
}
__device__ float wsum(float v){for(int o=32;o;o>>=1)v+=__shfl_xor(v,o);return v;}
__device__ float wmax(float v){for(int o=32;o;o>>=1)v=fmaxf(v,__shfl_xor(v,o));return v;}
__global__ void ln_k(const u16*x,const float*mod,u16*h,int domod){
  int row=blockIdx.x*4+(threadIdx.x>>6),lane=threadIdx.x&63; int b=row/N_;
  float v[18];float s=0;for(int i=0;i<18;i++){v[i]=bf2f(x[(long)row*D_+i*64+lane]);s+=v[i];}
  float m=wsum(s)/D_;float q=0;for(int i=0;i<18;i++){float d=v[i]-m;q+=d*d;}
  float r=rsqrtf(wsum(q)/D_+1e-5f);
  for(int i=0;i<18;i++){int d=i*64+lane;float y=(v[i]-m)*r; if(domod)y=y*(1.f+mod[b*3*D_+D_+d])+mod[b*3*D_+d]; h[(long)row*D_+d]=f2bf(y);}
}
__global__ void sm_k(const float*S,u16*P,int n,int ld,int ldp){
  int row=blockIdx.x*4+(threadIdx.x>>6),lane=threadIdx.x&63;
  float m=-1e30f;for(int i=lane;i<n;i+=64)m=fmaxf(m,S[(long)row*ld+i]);m=wmax(m);
  float s=0;for(int i=lane;i<n;i+=64)s+=__expf(S[(long)row*ld+i]-m);s=wsum(s);
  for(int i=lane;i<n;i+=64)P[(long)row*ldp+i]=f2bf(__expf(S[(long)row*ld+i]-m)/s);
}
__global__ void res_k(const u16*x,const float*o,const float*mod,u16*y,int gate){
  long i=(long)blockIdx.x*256+threadIdx.x; int b=i/((long)N_*D_),d=i%D_;
  float g=gate?mod[b*3*D_+2*D_+d]:1.f; y[i]=f2bf(bf2f(x[i])+g*o[i]);
}
__global__ __launch_bounds__(256) void probe_k(int*sink){
  unsigned long long t0=__builtin_amdgcn_s_memrealtime();
  asm volatile("s_waitcnt lgkmcnt(0)");
  float v=threadIdx.x;
  asm volatile(".rept 2048\n v_fma_f32 %0, %0, %0, %0\n .endr" : "+v"(v));
  if(v==1.2345f) sink[0]=1;

  unsigned long long t1=__builtin_amdgcn_s_memrealtime();
  unsigned long long dt=(t1-t0)*1000;
  while(__builtin_amdgcn_s_memrealtime()-t1<dt) __builtin_amdgcn_s_sleep(32);
}

__global__ __launch_bounds__(256) void pref_k(int*sink){
  unsigned long long pc;
  asm volatile("s_getpc_b64 s[40:41]\n s_add_u32 s40, s40, _Z7probe_kPi@rel32@lo+4\n s_addc_u32 s41, s41, _Z7probe_kPi@rel32@hi+12\n s_mov_b64 %0, s[40:41]":"=s"(pc)::"s40","s41","scc");
  const int* a=(const int*)((const char*)pc+threadIdx.x*64);
  int pf=a[0]+a[64];
  if(pf==0x12345677) sink[0]=1;
}
extern "C" void kernel_launch(void* const* d_in,const int* in_sizes,int n_in,void* d_out,int out_size,void* d_ws,size_t ws_size,hipStream_t stream){
  const u16*x=(const u16*)d_in[0],*c=(const u16*)d_in[1],*y=(const u16*)d_in[2],*wqkv=(const u16*)d_in[3],*wo=(const u16*)d_in[4],*wqc=(const u16*)d_in[5],*wkvc=(const u16*)d_in[6],*woc=(const u16*)d_in[7],*wm=(const u16*)d_in[8];
  char*ws=(char*)d_ws; size_t o=0; auto al=[&](size_t b){void*p=ws+o;o+=(b+255)/256*256;return p;};
  float*mod=(float*)al(2*3*D_*4); u16*h=(u16*)al(2048L*D_*2); u16*Wqk=(u16*)al((long)D_*D_*2),*Wvo=(u16*)al((long)D_*D_*2);
  u16*T=(u16*)al(2048L*D_*2),*V=(u16*)al(2048L*D_*2); float*S=(float*)al(2048L*1024*4); u16*P=(u16*)al(2048L*1024*2);
  float*O=(float*)al(2048L*D_*4); u16*x1=(u16*)al(2048L*D_*2),*hc=(u16*)al(2048L*D_*2); u16*kvc=(u16*)al(154L*2*D_*2);
  u16*Wqc=(u16*)al(2L*D_*80*2),*Voc=(u16*)al(2L*80*D_*2); float*Sc=(float*)al(2048L*80*4); u16*Pc=(u16*)al(2048L*80*2);
  float sc=1.f/sqrtf((float)D_);
  auto G=[&](const u16*A,long sam,long sak,long bsA,const u16*Bm,long sbk,long sbn,long bsB,void*C,long scm,long bsC,int M,int N,int K,int nb,int mode,float s){
    gemm_g<<<dim3((N+15)/16,(M+63)/64,nb),256,0,stream>>>(A,sam,sak,bsA,Bm,sbk,sbn,bsB,C,scm,bsC,M,N,K,mode,s);};
  pref_k<<<256,256,0,stream>>>((int*)(ws+ws_size-64));
  probe_k<<<256,256,0,stream>>>((int*)(ws+ws_size-64));
  mod_k<<<(3*D_+255)/256,256,0,stream>>>(c,wm,mod);
  ln_k<<<512,256,0,stream>>>(x,mod,h,1);
  G(wqkv,3*D_,1,0,wqkv+D_,1,3*D_,0,Wqk,D_,0,D_,D_,D_,1,0,1.f);
  G(wqkv+2*D_,3*D_,1,0,wo,D_,1,0,Wvo,D_,0,D_,D_,D_,1,0,1.f);
  G(h,D_,1,0,Wqk,D_,1,0,T,D_,0,2048,D_,D_,1,0,1.f);
  G(h,D_,1,0,Wvo,D_,1,0,V,D_,0,2048,D_,D_,1,0,1.f);
  G(T,D_,1,1024L*D_,h,1,D_,1024L*D_,S,1024,1024L*1024,1024,1024,D_,2,1,sc);
  sm_k<<<512,256,0,stream>>>(S,P,1024,1024,1024);
  G(P,1024,1,1024L*1024,V,D_,1,1024L*D_,O,D_,1024L*D_,1024,D_,1024,2,1,1.f);
  res_k<<<2048*D_/256,256,0,stream>>>(x,O,mod,x1,1);
  ln_k<<<512,256,0,stream>>>(x1,mod,hc,0);
  G(y,D_,1,0,wkvc,2*D_,1,0,kvc,2*D_,0,154,2*D_,D_,1,0,1.f);
  G(wqc,D_,1,0,kvc,1,2*D_,77L*2*D_,Wqc,80,(long)D_*80,D_,77,D_,2,0,1.f);
  G(kvc+D_,2*D_,1,77L*2*D_,woc,D_,1,0,Voc,D_,80L*D_,77,D_,D_,2,0,1.f);
  G(hc,D_,1,1024L*D_,Wqc,80,1,(long)D_*80,Sc,80,1024L*80,1024,77,D_,2,1,sc);
  sm_k<<<512,256,0,stream>>>(Sc,Pc,77,80,80);
  G(Pc,80,1,1024L*80,Voc,D_,1,80L*D_,O,D_,1024L*D_,1024,D_,77,2,1,1.f);
  res_k<<<2048*D_/256,256,0,stream>>>(x1,O,mod,(u16*)d_out,0);
}
```

```cpp
#include <hip/hip_runtime.h>
#include <hip/hip_bf16.h>
#include <type_traits>

typedef __attribute__((ext_vector_type(8))) __bf16 bf16x8;
typedef __attribute__((ext_vector_type(2))) __bf16 bf16x2;
typedef __attribute__((ext_vector_type(4))) float f32x4;
typedef __attribute__((ext_vector_type(2))) float f32x2;
typedef __attribute__((ext_vector_type(4))) unsigned u32x4;
typedef __attribute__((ext_vector_type(2))) unsigned u32x2;
typedef unsigned long long u64;
#define LDS __attribute__((address_space(3)))
#define GAS __attribute__((address_space(1)))
#define DI __device__ __forceinline__

constexpr int DM = 1152;
constexpr int KD = 36;
constexpr int BLK = 1024;
constexpr size_t O_FLAG = 0;
constexpr size_t O_MOD = 4096;
constexpr size_t O_H = 32768;
constexpr size_t SZ_ACT = 128ull * 36 * 1024;
constexpr size_t O_W = O_H + SZ_ACT;
constexpr size_t O_T = O_W + 144ull * 36 * 1024;
constexpr size_t O_V = O_T + SZ_ACT;
constexpr size_t O_P = O_V + SZ_ACT;
constexpr size_t O_ST = O_P + 128ull * 32 * 1024;
constexpr size_t O_X1 = O_ST + 128ull * 16 * 16 * 8;
constexpr size_t SZ_TOK = 10ull * 36 * 1024;
constexpr size_t O_KC = O_X1 + SZ_ACT;
constexpr size_t O_VC = O_KC + SZ_TOK;
constexpr size_t O_WQC = O_VC + SZ_TOK;
constexpr size_t O_VOC = O_WQC + SZ_TOK;
constexpr size_t WS_END = O_VOC + SZ_TOK;

constexpr float QK_SCALE = 1.4426950408889634f * 0.029462782549439476f;

#if defined(__HIP_DEVICE_COMPILE__)
typedef __amdgpu_buffer_rsrc_t rsrc_t;
DI rsrc_t mk_rsrc(const void* p, unsigned bytes) { return __builtin_amdgcn_make_buffer_rsrc((void*)p, 0, bytes, 0x00020000); }
DI unsigned pk2(float a, float b) { f32x2 p = {a, b}; return __builtin_bit_cast(unsigned, __builtin_convertvector(p, bf16x2)); }
DI float blo(unsigned u) { return __uint_as_float(u << 16); }
DI float bhi(unsigned u) { return __uint_as_float(u & 0xffff0000u); }
template <int N> DI void wait_vm() { asm volatile("s_waitcnt vmcnt(%0)" ::"n"(N > 63 ? 63 : N) : "memory"); }
template <int N> DI void wait_lgkm() { asm volatile("s_waitcnt lgkmcnt(%0)" ::"n"(N > 15 ? 15 : N) : "memory"); }
DI void SB() { __builtin_amdgcn_sched_barrier(0); }
DI void bar() { __builtin_amdgcn_s_barrier(); }
template <int I, int N, class F> DI void sfor(F&& f) { if constexpr (I < N) { f(std::integral_constant<int, I>{}); sfor<I + 1, N>(f); } }
template <int V> using ic = std::integral_constant<int, V>;
DI void mfma(f32x4& c, const u32x4& x, const u32x4& y) { asm volatile("v_mfma_f32_16x16x32_bf16 %0, %1, %2, %0" : "+v"(c) : "v"(x), "v"(y)); }
template <int OFF> DI void dsr128(u32x4& d, unsigned a) { asm volatile("ds_read_b128 %0, %1 offset:%2" : "=v"(d) : "v"(a), "n"(OFF)); }
template <int OFF> DI void dsr_tr(u32x2& d, unsigned a) { asm volatile("ds_read_b64_tr_b16 %0, %1 offset:%2" : "=v"(d) : "v"(a), "n"(OFF)); }
DI u32x4 ld128(rsrc_t r, unsigned v, unsigned s) { return __builtin_amdgcn_raw_buffer_load_b128(r, v, s, 0); }
DI u32x2 ld64(rsrc_t r, unsigned v, unsigned s) { return __builtin_amdgcn_raw_buffer_load_b64(r, v, s, 0); }
template <int AUX = 0> DI void st128(u32x4 d, rsrc_t r, unsigned v, unsigned s) {
  __builtin_amdgcn_raw_buffer_store_b128(d, r, v, s, AUX); SB(); asm volatile("s_nop 1"); SB();
}
DI void st64(u32x2 d, rsrc_t r, unsigned v, unsigned s) { __builtin_amdgcn_raw_buffer_store_b64(d, r, v, s, 0); }
template <int IMM> DI void dma16(rsrc_t r, LDS char* d, unsigned v, unsigned s) { __builtin_amdgcn_raw_ptr_buffer_load_lds(r, d, 16, v, s, IMM, 0); }
DI u32x4 pack8(const f32x4& a, const f32x4& b) { return u32x4{pk2(a[0], a[1]), pk2(a[2], a[3]), pk2(b[0], b[1]), pk2(b[2], b[3])}; }
DI u32x2 pack4(const f32x4& a) { return u32x2{pk2(a[0], a[1]), pk2(a[2], a[3])}; }
DI void mfma_done() { asm volatile("s_nop 15" ::: "memory"); }
template <class T> DI void pin(T& x) { asm volatile("" : "+v"(x)); }
DI float qmax(float x) {
  auto a = __builtin_amdgcn_permlane32_swap(__float_as_uint(x), __float_as_uint(x), false, false);
  x = fmaxf(__uint_as_float(a[0]), __uint_as_float(a[1]));
  auto b = __builtin_amdgcn_permlane16_swap(__float_as_uint(x), __float_as_uint(x), false, false);
  return fmaxf(__uint_as_float(b[0]), __uint_as_float(b[1]));
}
DI float qsum(float x) {
  auto a = __builtin_amdgcn_permlane32_swap(__float_as_uint(x), __float_as_uint(x), false, false);
  x = __uint_as_float(a[0]) + __uint_as_float(a[1]);
  auto b = __builtin_amdgcn_permlane16_swap(__float_as_uint(x), __float_as_uint(x), false, false);
  return __uint_as_float(b[0]) + __uint_as_float(b[1]);
}
constexpr unsigned OOB = 0x7ff00000u;

constexpr int FR = 5;
constexpr int FPRE = 3;
template <int NT, int KS>
DI void f_loader(rsrc_t rb, unsigned boff, char* shm, int lane) {
  constexpr int G = KS / 2, SLOT = NT * 2048;
  unsigned voff[NT];
#pragma unroll
  for (int n = 0; n < NT; n++) voff[n] = boff + lane * 16 + n * KS * BLK - (n & 1) * 2048;
  auto issue = [&](int g, int slot) {
    unsigned so = g * 2048;
    LDS char* base = (LDS char*)shm + slot * SLOT;
    sfor<0, NT>([&](auto i) {
      constexpr int n = decltype(i)::value;
      dma16<(n & 1) * 2048>(rb, base + (n >> 1) * 4096, voff[n], so);
      dma16<(n & 1) * 2048 + 1024>(rb, base + (n >> 1) * 4096, voff[n], so);
    });
  };
  issue(0, 0); issue(1, 1);
  wait_vm<2 * NT>(); bar();
  issue(2, 2); issue(3, 3);
  int slot = 4;
#pragma unroll 1
  for (int g = 1; g < G - FPRE; g++) {
    wait_vm<4 * NT>(); bar();
    issue(g + FPRE, slot); slot = (slot + 1 == FR) ? 0 : slot + 1;
  }
  wait_vm<4 * NT>(); bar();
  wait_vm<2 * NT>(); bar();
  wait_vm<0>(); bar();
  bar();
}

template <int NT, int KS, int D, bool SWAP, bool SCALE>
DI void f_consumer(rsrc_t ra, unsigned a0, unsigned a1, const char* shm, int lane, f32x4 (&acc)[2][NT], unsigned tab) {
  constexpr int G = KS / 2, SLOT = NT * 2048;
  static_assert(G % (D / 2) == 0, "");
  u32x4 a[D][2];
#pragma unroll
  for (int d = 0; d < D - 1; d++) { a[d][0] = ld128(ra, a0, d * BLK); SB(); a[d][1] = ld128(ra, a1, d * BLK); SB(); }
  u32x4 b0[NT], b1[NT];
  unsigned lp = (unsigned)(size_t)(const LDS char*)shm + lane * 16;
  unsigned sh0 = 0, sh1 = 0;
  auto half = [&](u32x4 (&av)[2], u32x4 (&bc)[NT], u32x4 (&bn)[NT], unsigned addr, auto hoff, u32x4 (&ar)[2], unsigned so, auto nextra) {
    constexpr int NX = decltype(nextra)::value;
    if constexpr (SCALE) {
      if constexpr (NX) wait_lgkm<0>();
#pragma unroll
      for (int j = 0; j < 4; j++) {
        asm volatile("v_pk_sub_u16 %0, %0, %1 clamp" : "+v"(av[0][j]) : "v"(sh0));
        asm volatile("v_pk_sub_u16 %0, %0, %1 clamp" : "+v"(av[1][j]) : "v"(sh1));
      }
      asm volatile("s_nop 1");
    }
    sfor<0, NT>([&](auto i) {
      constexpr int I = decltype(i)::value, WS = 3;
      if constexpr (I % WS == 0) { constexpr int need = (I + WS < NT ? I + WS : NT); wait_lgkm<NT - need + I>(); }
      if constexpr (SWAP) mfma(acc[0][I], av[0], bc[I]); else mfma(acc[0][I], bc[I], av[0]);
      dsr128<I * 2048 + decltype(hoff)::value>(bn[I], addr);
      if constexpr (SWAP) mfma(acc[1][I], av[1], bc[I]); else mfma(acc[1][I], bc[I], av[1]);
      if constexpr (I == 0) { SB(); ar[0] = ld128(ra, a0, so); SB(); }
      if constexpr (I == 2) { SB(); ar[1] = ld128(ra, a1, so); SB(); }
    });
  };
#pragma unroll
  for (int n = 0; n < NT; n++) { pin(acc[0][n]); pin(acc[1][n]); }
  SB();
  bar();
  sfor<0, NT>([&](auto i) { dsr128<decltype(i)::value * 2048>(b0[decltype(i)::value], lp); });
  int slot = 0; unsigned cur = lp;
#pragma unroll 1
  for (int g0 = 0; g0 < G; g0 += D / 2) {
#pragma unroll
    for (int u = 0; u < D / 2; u++) {
      int g = g0 + u;
      if constexpr (SCALE) {
        asm volatile("ds_read_b32 %0, %2\n\tds_read_b32 %1, %2 offset:1024" : "=&v"(sh0), "=&v"(sh1) : "v"(tab + g * 64));
      }
      unsigned so0 = (2 * g - 1 + D < KS) ? (2 * g - 1 + D) * BLK : OOB;
      half(a[2 * u], b0, b1, cur, ic<1024>{}, a[(2 * u + D - 1) % D], so0, ic<1>{});
      slot = (slot + 1 == FR) ? 0 : slot + 1; cur = lp + slot * SLOT;
      unsigned so1 = (2 * g + D < KS) ? (2 * g + D) * BLK : OOB;
      bar();
      half(a[2 * u + 1], b1, b0, cur, ic<0>{}, a[2 * u], so1, ic<0>{});
    }
  }
  wait_lgkm<0>();
  mfma_done();
#pragma unroll
  for (int n = 0; n < NT; n++) { pin(acc[0][n]); pin(acc[1][n]); }
}

enum { KC = 0, TR = 1, FL = 2 };
constexpr int RR = 4;
template <int NP>
DI void r_loader(rsrc_t r, const unsigned (&voff)[NP], unsigned kstep, int rot, char* shm, int slot_bytes, int ioff, bool pretouch) {
  constexpr int G = 18;
  auto issue = [&](int g, int slot) {
    int gg = g + rot; gg = gg >= G ? gg - G : gg;
    unsigned so = gg * kstep;
    LDS char* base = (LDS char*)shm + slot * slot_bytes + ioff;
    sfor<0, NP>([&](auto i) { constexpr int p = decltype(i)::value; dma16<(p & 3) * 1024>(r, base + (p >> 2) * 4096, voff[p], so); });
  };
  if (pretouch) {
#pragma unroll 1
    for (int g = 3; g < G; g++) issue(g, 3);
  }
  issue(0, 0); issue(1, 1);
  wait_vm<NP>(); bar();
  issue(2, 2); issue(3, 3);
  int slot = 0;
#pragma unroll 1
  for (int g = 1; g < G - 3; g++) {
    wait_vm<2 * NP>(); bar();
    issue(g + 3, slot); slot = (slot + 1) & 3;
  }
  wait_vm<2 * NP>(); bar();
  wait_vm<NP>(); bar();
  wait_vm<0>(); bar();
  bar();
}
template <int NP> DI void voff_kc(unsigned (&v)[NP], unsigned base, unsigned stride, int lane) {
#pragma unroll
  for (int p = 0; p < NP; p++) { int row = 8 * p + (lane >> 3); v[p] = base + row * stride + (((lane & 7) ^ ((row >> 1) & 7)) << 4) - (p & 3) * 1024; }
}
template <int NP> DI void voff_tr(unsigned (&v)[NP], unsigned base, unsigned stride, int lane) {
#pragma unroll
  for (int p = 0; p < NP; p++) {
    int row = 8 * (p & 7) + (lane >> 3), f = ((row >> 1) & 1) | (((row >> 3) & 1) << 1);
    v[p] = base + (p >> 3) * 128 + row * stride + (((lane & 7) ^ (f << 1)) << 4) - (p & 3) * 1024;
  }
}
template <int KIND, int NTL> struct Frag {
  unsigned base[KIND == TR ? NTL : (KIND == KC ? 2 : 1)];
  DI void init(unsigned lds0, int ioff, int t0, int lane) {
    int r = lane & 15, q = lane >> 4;
    if constexpr (KIND == KC) {
      unsigned b = lds0 + ioff + (t0 * 16 + r) * 128;
      base[0] = b + ((q ^ ((r >> 1) & 7)) << 4); base[1] = b + (((4 + q) ^ ((r >> 1) & 7)) << 4);
    } else if constexpr (KIND == TR) {
      int qq = r >> 2, p = r & 3, f = ((qq >> 1) & 1) | ((q & 1) << 1);
#pragma unroll
      for (int c = 0; c < NTL; c++) { int t = t0 + c; base[c] = lds0 + ioff + (t >> 2) * 8192 + (8 * q + qq) * 128 + (((t & 3) ^ f) << 5) + p * 8; }
    } else base[0] = lds0 + ioff + t0 * 2048 + lane * 16;
  }
  static constexpr int NRD = KIND == TR ? 2 : 1;
  template <int H, int I> DI void read(u32x4& d, unsigned so) const {
    if constexpr (KIND == KC) dsr128<I * 2048>(d, base[H] + so);
    else if constexpr (KIND == FL) dsr128<I * 2048 + H * 1024>(d, base[0] + so);
    else {
      u32x2 lo, hi;
      dsr_tr<H * 4096>(lo, base[I] + so); dsr_tr<H * 4096 + 512>(hi, base[I] + so);
      d = u32x4{lo[0], lo[1], hi[0], hi[1]};
    }
  }
};
template <int KX, int NX, int KY, int NY>
DI void r_consumer(const Frag<KX, NX>& fx, const Frag<KY, NY>& fy, int slot_bytes, f32x4 (&acc)[NX][NY]) {
  constexpr int G = 18;
  u32x4 x0[NX], y0[NY], x1[NX], y1[NY];
  auto half = [&](u32x4 (&xc)[NX], u32x4 (&yc)[NY], u32x4 (&xn)[NX], u32x4 (&yn)[NY], unsigned so, auto hh) {
    constexpr int H = decltype(hh)::value;
    sfor<0, NX * NY>([&](auto i) {
      constexpr int I = decltype(i)::value;
      mfma(acc[I / NY][I % NY], xc[I / NY], yc[I % NY]);
      if constexpr (I < NY) fy.template read<H, I>(yn[I], so);
      else if constexpr (I < NX + NY) fx.template read<H, I - NY>(xn[I - NY], so);
    });
    wait_lgkm<0>();
#pragma unroll
    for (int i = 0; i < NX; i++) pin(xn[i]);
#pragma unroll
    for (int i = 0; i < NY; i++) pin(yn[i]);
  };
#pragma unroll
  for (int i = 0; i < NX; i++)
#pragma unroll
    for (int j = 0; j < NY; j++) pin(acc[i][j]);
  SB();
  bar();
  sfor<0, NY>([&](auto i) { fy.template read<0, decltype(i)::value>(y0[decltype(i)::value], 0); });
  sfor<0, NX>([&](auto i) { fx.template read<0, decltype(i)::value>(x0[decltype(i)::value], 0); });
  wait_lgkm<0>();
#pragma unroll
  for (int i = 0; i < NX; i++) pin(x0[i]);
#pragma unroll
  for (int i = 0; i < NY; i++) pin(y0[i]);
  int slot = 0; unsigned so = 0;
#pragma unroll 1
  for (int g = 0; g < G; g++) {
    half(x0, y0, x1, y1, so, ic<1>{});
    slot = (slot + 1) & 3; so = slot * slot_bytes;
    bar();
    half(x1, y1, x0, y0, so, ic<0>{});
  }
  mfma_done();
#pragma unroll
  for (int i = 0; i < NX; i++)
#pragma unroll
    for (int j = 0; j < NY; j++) pin(acc[i][j]);
}
template <int NX, int NY> DI void zero(f32x4 (&a)[NX][NY]) {
#pragma unroll
  for (int i = 0; i < NX; i++)
#pragma unroll
    for (int j = 0; j < NY; j++) a[i][j] = f32x4{0, 0, 0, 0};
}
DI u64 mix64(u64 z) { z += 0x9E3779B97F4A7C15ull; z = (z ^ (z >> 30)) * 0xBF58476D1CE4E5B9ull; z = (z ^ (z >> 27)) * 0x94D049BB133111EBull; return z ^ (z >> 31); }

template <int MODE>
DI void tok_gemm(const char* xsrc, const char* wsrc, char* ws, int pn, char* shm, int wid, int lane, int ldr0) {
  constexpr int SLOT = 28 * 1024, WOFF = 20 * 1024;
  int rot = pn % 18;
  if (wid == ldr0) {
    unsigned v[20];
    if constexpr (MODE == 0) {
#pragma unroll
      for (int p = 0; p < 20; p++) {
        int row = 8 * p + (lane >> 3), b = row >= 80, t = row - 80 * b;
        v[p] = t < 77 ? (b * 77 + t) * 2304 + (((lane & 7) ^ ((row >> 1) & 7)) << 4) - (p & 3) * 1024 : OOB;
      }
      r_loader<20>(mk_rsrc(xsrc, 154 * 2304), v, 128, rot, shm, SLOT, 0, false);
    } else {
#pragma unroll
      for (int p = 0; p < 20; p++) v[p] = (p >> 1) * KD * BLK + (p & 1) * BLK + lane * 16 - (p & 3) * 1024;
      r_loader<20>(mk_rsrc(xsrc, SZ_TOK), v, 2048, rot, shm, SLOT, 0, false);
    }
  } else if (wid == ldr0 + 1) {
    unsigned v[8];
    if constexpr (MODE == 0) { voff_tr<8>(v, pn * 128, 4608, lane); r_loader<8>(mk_rsrc(wsrc, DM * 4608), v, 64 * 4608, rot, shm, SLOT, WOFF, true); }
    else if constexpr (MODE == 1) { voff_kc<8>(v, pn * 64 * 2304, 2304, lane); r_loader<8>(mk_rsrc(wsrc, DM * 2304), v, 128, rot, shm, SLOT, WOFF, false); }
    else { voff_tr<8>(v, pn * 128, 2304, lane); r_loader<8>(mk_rsrc(wsrc, DM * 2304), v, 64 * 2304, rot, shm, SLOT, WOFF, false); }
  } else {
    unsigned l0 = (unsigned)(size_t)(LDS char*)shm;
    int b = wid >> 1, wn = wid & 1, r = lane & 15, q = lane >> 4;
    constexpr int KT = MODE == 0 ? KC : FL, KW = MODE == 1 ? KC : TR;
    Frag<KT, 5> ft; ft.init(l0, 0, b * 5, lane);
    Frag<KW, 2> fw; fw.init(l0, WOFF, wn * 2, lane);
    if constexpr (MODE == 2) {
      f32x4 acc[5][2]; zero(acc);
      r_consumer(ft, fw, SLOT, acc);
      rsrc_t ro = mk_rsrc(ws + O_VOC, SZ_TOK);
#pragma unroll
      for (int j = 0; j < 2; j++) {
        unsigned o = (b * 72 + pn * 4 + wn * 2 + j) * 2560;
        st128(pack8(acc[0][j], acc[1][j]), ro, lane * 16, o);
        st128(pack8(acc[2][j], acc[3][j]), ro, lane * 16 + 1024, o);
        st64(pack4(acc[4][j]), ro, lane * 8 + 2048, o);
      }
    } else {
      f32x4 acc[2][5]; zero(acc);
      r_consumer(fw, ft, SLOT, acc);
      if constexpr (MODE == 0) {
        rsrc_t ro = mk_rsrc(ws + O_KC, 2 * SZ_TOK);
        int kb = pn * 2 + wn, sel = kb >= KD;
        unsigned o = sel * SZ_TOK + (kb - sel * KD) * BLK;
        unsigned vo = ((q >> 1) * 16 + r) * 16 + (q & 1) * 8;
#pragma unroll
        for (int t = 0; t < 5; t++) {
          st64(pack4(acc[0][t]), ro, vo, o + (b * 5 + t) * KD * BLK);
          st64(pack4(acc[1][t]), ro, vo + 512, o + (b * 5 + t) * KD * BLK);
        }
      } else {
        rsrc_t ro = mk_rsrc(ws + O_WQC, SZ_TOK);
#pragma unroll
        for (int t = 0; t < 5; t++) st128(pack8(acc[0][t] * QK_SCALE, acc[1][t] * QK_SCALE), ro, lane * 16, ((b * 5 + t) * KD + pn * 2 + wn) * BLK);
      }
    }
  }
}
#endif

constexpr int K0_LDS = 147456 + 4096;
__global__ __launch_bounds__(384) void k0_pre(const char* x, const char* c, const char* y, const char* wqkv, const char* wo,
                                             const char* wkvc, const char* wmod, char* ws) {
#if defined(__HIP_DEVICE_COMPILE__)
  extern __shared__ __attribute__((aligned(16))) char shm[];
  int bid = blockIdx.x, xcd = bid & 7, idx = bid >> 3;
  int wid = __builtin_amdgcn_readfirstlane(threadIdx.x >> 6), lane = threadIdx.x & 63;
  int qd = xcd & 3, nr = (qd & 1) ? 4 : 5, nc = (qd & 2) ? 4 : 5, r0 = (qd & 1) ? 5 : 0, c0 = (qd & 2) ? 5 : 0;
  if (idx < nr * nc) {
    int ti = r0 + idx % nr, tj = c0 + idx / nr, mat = xcd >> 2;
    int rot = 2 * ((ti + tj) % 9);
    constexpr int SLOT = 32768;
    if (wid == 4) {
      unsigned v[16]; voff_kc<16>(v, ti * 128 * 6912 + mat * 4608, 6912, lane);
      r_loader<16>(mk_rsrc(wqkv, DM * 6912), v, 128, rot, shm, SLOT, 0, false);
    } else if (wid == 5) {
      unsigned v[16];
      if (mat == 0) { voff_kc<16>(v, tj * 128 * 6912 + 2304, 6912, lane); r_loader<16>(mk_rsrc(wqkv, DM * 6912), v, 128, rot, shm, SLOT, 16384, false); }
      else { voff_tr<16>(v, tj * 256, 2304, lane); r_loader<16>(mk_rsrc(wo, DM * 2304), v, 64 * 2304, rot, shm, SLOT, 16384, false); }
    } else {
      unsigned l0 = (unsigned)(size_t)(LDS char*)shm;
      int wi = wid >> 1, wj = wid & 1;
      f32x4 acc[4][4]; zero(acc);
      Frag<KC, 4> fx; fx.init(l0, 0, wi * 4, lane);
      float sc = 1.f;
      if (mat == 0) { Frag<KC, 4> fy; fy.init(l0, 16384, wj * 4, lane); r_consumer(fx, fy, SLOT, acc); sc = QK_SCALE; }
      else { Frag<TR, 4> fy; fy.init(l0, 16384, wj * 4, lane); r_consumer(fx, fy, SLOT, acc); }
      rsrc_t ro = mk_rsrc(ws + O_W, 144 * KD * BLK);
      unsigned o = ((mat * 72 + tj * 8 + wj * 4) * KD + ti * 4 + wi * 2) * BLK;
#pragma unroll
      for (int j = 0; j < 4; j++) {
        st128(pack8(acc[0][j] * sc, acc[1][j] * sc), ro, lane * 16, o + j * KD * BLK);
        st128(pack8(acc[2][j] * sc, acc[3][j] * sc), ro, lane * 16 + 1024, o + j * KD * BLK);
      }
    }
    return;
  }
  int ob = (xcd >> 2) * 47 + (qd == 0 ? 0 : qd == 1 ? 7 : qd == 2 ? 19 : 31);
  int o = ob + idx - nr * nc;
  if (o < 72 && !(o & 1)) { tok_gemm<0>(y, wkvc, ws, o >> 1, shm, wid, lane, 4); return; }
  int ax = o < 72 ? o >> 1 : o - 36;
  GAS u64* flg = (GAS u64*)(ws + O_FLAG);
  u64 ep = __hip_atomic_load(flg, __ATOMIC_RELAXED, __HIP_MEMORY_SCOPE_AGENT);
  int r = lane & 15, q = lane >> 4;
  unsigned l0 = (unsigned)(size_t)(LDS char*)shm;
  if (ax < 54) {
    rsrc_t rw = mk_rsrc(wmod, DM * 13824);
    unsigned v[8]; voff_tr<8>(v, ax * 128 + wid * 192 * 13824, 13824, lane);
    LDS char* base = (LDS char*)shm + wid * 24576;
#pragma unroll
    for (int g = 0; g < 3; g++) sfor<0, 8>([&](auto i) { constexpr int p = decltype(i)::value; dma16<(p & 3) * 1024>(rw, base + g * 8192 + (p >> 2) * 4096, v[p], g * 64 * 13824); });
  }
  SB();
  u32x4 cf[6];
  { rsrc_t rc = mk_rsrc(c, 2 * 2304);
    unsigned vo = r < 2 ? r * 2304 + wid * 384 + q * 16 : OOB;
#pragma unroll
    for (int s = 0; s < 6; s++) cf[s] = ld128(rc, vo, s * 64); }
  SB();
  int ht = (ax - 15) * 3 + (wid >> 1), kh = wid & 1;
  bool hval = ax >= 15 && ht < 128;
  u32x4 xv[18];
  { rsrc_t rx = mk_rsrc(x, 2048 * 2304);
    unsigned vo = hval ? (ht * 16 + r) * 2304 + kh * 1152 + q * 16 : OOB;
#pragma unroll
    for (int s = 0; s < 18; s++) xv[s] = ld128(rx, vo, s * 64); }
  SB();
  u64 tag = mix64(ep);
  LDS float* red = (LDS float*)(shm + 147456);
  if (ax < 54) {
#pragma unroll
    for (int s = 0; s < 6; s++)
#pragma unroll
      for (int j = 0; j < 4; j++) {
        float a = blo(cf[s][j]), b = bhi(cf[s][j]);
        cf[s][j] = pk2(a / (1.f + __expf(-a)), b / (1.f + __expf(-b)));
      }
    wait_vm<18>();
    Frag<TR, 4> fw; fw.init(l0, wid * 24576, 0, lane);
    f32x4 acc[4] = {};
#pragma unroll
    for (int j = 0; j < 4; j++) pin(acc[j]);
    sfor<0, 6>([&](auto i) {
      constexpr int s = decltype(i)::value;
      u32x4 w[4];
      sfor<0, 4>([&](auto jj) { fw.template read<s & 1, decltype(jj)::value>(w[decltype(jj)::value], (s >> 1) * 8192); });
      wait_lgkm<0>();
#pragma unroll
      for (int j = 0; j < 4; j++) pin(w[j]);
      pin(cf[s]);
      asm volatile("s_nop 1");
#pragma unroll
      for (int j = 0; j < 4; j++) mfma(acc[j], w[j], cf[s]);
    });
    mfma_done();
#pragma unroll
    for (int j = 0; j < 4; j++) pin(acc[j]);
    if (r < 2) {
#pragma unroll
      for (int j = 0; j < 4; j++) *(LDS f32x4*)(red + wid * 128 + r * 64 + j * 16 + q * 4) = acc[j];
    }
  }
  LDS float* st = (LDS float*)(shm + 147456 + 3072);
  float s1 = 0;
#pragma unroll
  for (int s = 0; s < 18; s++)
#pragma unroll
    for (int j = 0; j < 4; j++) s1 += blo(xv[s][j]) + bhi(xv[s][j]);
  s1 = qsum(s1);
  if (q == 0) st[wid * 16 + r] = s1;
  wait_lgkm<0>(); bar();
  if (wid == 0) {
    if (ax < 54 && lane < 16) {
      f32x4 a = {}, b = {};
#pragma unroll
      for (int w = 0; w < 6; w++) { a += *(LDS f32x4*)(red + w * 128 + lane * 8); b += *(LDS f32x4*)(red + w * 128 + lane * 8 + 4); }
      st128<16>(pack8(a, b), mk_rsrc(ws + O_MOD, 13824), (lane >> 3) * 6912 + ax * 128 + (lane & 7) * 16, 0);
    }
    wait_vm<0>();
    if (lane == 0) __hip_atomic_store(flg + 8 + ax, tag, __ATOMIC_RELAXED, __HIP_MEMORY_SCOPE_AGENT);
  }
  float mean = (st[(wid & ~1) * 16 + r] + st[(wid | 1) * 16 + r]) * (1.f / DM);
  float s2 = 0;
#pragma unroll
  for (int s = 0; s < 18; s++)
#pragma unroll
    for (int j = 0; j < 4; j++) { float a = blo(xv[s][j]) - mean, b = bhi(xv[s][j]) - mean; s2 += a * a + b * b; }
  s2 = qsum(s2);
  if (q == 0) st[96 + wid * 16 + r] = s2;
  if (ax < 15) return;
  if (wid == 0) {
    int n = ax == 57 ? 58 : 36;
    for (int spin = 0; spin < (1 << 20); spin++) {
      u64 f = __hip_atomic_load(flg + 8 + (lane < n ? lane : 0), __ATOMIC_RELAXED, __HIP_MEMORY_SCOPE_AGENT);
      if (__all(f == tag)) break;
      __builtin_amdgcn_s_sleep(2);
    }
    if (ax == 57 && lane == 0) __hip_atomic_store(flg, tag, __ATOMIC_RELAXED, __HIP_MEMORY_SCOPE_AGENT);
  }
  wait_lgkm<0>(); bar();
  float rstd = rsqrtf((st[96 + (wid & ~1) * 16 + r] + st[96 + (wid | 1) * 16 + r]) * (1.f / DM) + 1e-5f);
  {
    rsrc_t rm = mk_rsrc(ws + O_MOD, 13824), rh = mk_rsrc(ws + O_H, SZ_ACT);
    unsigned mo = (ht >> 6) * 6912 + kh * 1152 + q * 16;
    unsigned ho = hval ? (ht * KD + kh * 18) * BLK + ((((q & 1) << 1) | (q >> 1)) * 16 + r) * 16 : OOB;
#pragma unroll
    for (int s0 = 0; s0 < 18; s0 += 6) {
      u32x4 sf[6], sc[6];
#pragma unroll
      for (int s = 0; s < 6; s++) {
        sf[s] = __builtin_amdgcn_raw_buffer_load_b128(rm, mo, (s0 + s) * 64, 16);
        sc[s] = __builtin_amdgcn_raw_buffer_load_b128(rm, mo + 2304, (s0 + s) * 64, 16);
      }
#pragma unroll
      for (int s = 0; s < 6; s++) {
        unsigned hv[4];
#pragma unroll
        for (int j = 0; j < 4; j++) {
          unsigned xx = xv[s0 + s][j];
          hv[j] = pk2((blo(xx) - mean) * rstd * (1.f + blo(sc[s][j])) + blo(sf[s][j]), (bhi(xx) - mean) * rstd * (1.f + bhi(sc[s][j])) + bhi(sf[s][j]));
        }
        auto e = __builtin_amdgcn_permlane32_swap(hv[0], hv[2], false, false);
        auto f = __builtin_amdgcn_permlane32_swap(hv[1], hv[3], false, false);
        st128(u32x4{e[0], f[0], e[1], f[1]}, rh, ho, (s0 + s) * BLK);
      }
    }
  }
#endif
}

__global__ __launch_bounds__(320) void k2_tv(char* ws) {
#if defined(__HIP_DEVICE_COMPILE__)
  extern __shared__ __attribute__((aligned(16))) char shm[];
  constexpr int NT = 9;
  int bid = blockIdx.x, xcd = bid & 7, idx = bid >> 3;
  int rp = 4 * (xcd & 3) + (idx & 3), cp = 8 * (xcd >> 2) + (idx >> 2);
  int wid = __builtin_amdgcn_readfirstlane(threadIdx.x >> 6), lane = threadIdx.x & 63;
  if (wid == 4) { f_loader<NT, KD>(mk_rsrc(ws + O_W, 144 * KD * BLK), cp * 9 * KD * BLK, shm, lane); return; }
  rsrc_t ra = mk_rsrc(ws + O_H, SZ_ACT);
  unsigned a0 = (rp * 8 + wid * 2) * KD * BLK + lane * 16, a1 = a0 + KD * BLK;
  f32x4 acc[2][NT]; zero(acc);
  if (cp < 8) {
    asm volatile("; T" : "+v"(a0), "+v"(a1));
    f_consumer<NT, KD, 6, false, false>(ra, a0, a1, shm, lane, acc, 0);
    rsrc_t rt = mk_rsrc(ws + O_T, SZ_ACT);
    unsigned ob = (rp * 8 + wid * 2) * KD * BLK + lane * 16 + ((cp * 9) >> 1) * BLK;
    if (cp & 1) {
#pragma unroll
      for (int t = 0; t < 2; t++) {
        st64(pack4(acc[t][0]), rt, ob + 8, t * KD * BLK);
#pragma unroll
        for (int p = 0; p < 4; p++) st128(pack8(acc[t][1 + 2 * p], acc[t][2 + 2 * p]), rt, ob + (p + 1) * BLK, t * KD * BLK);
      }
    } else {
#pragma unroll
      for (int t = 0; t < 2; t++) {
#pragma unroll
        for (int p = 0; p < 4; p++) st128(pack8(acc[t][2 * p], acc[t][2 * p + 1]), rt, ob + p * BLK, t * KD * BLK);
        st64(pack4(acc[t][8]), rt, ob + 4 * BLK, t * KD * BLK);
      }
    }
  } else {
    asm volatile("; V" : "+v"(a0), "+v"(a1));
    f_consumer<NT, KD, 6, true, false>(ra, a0, a1, shm, lane, acc, 0);
    rsrc_t rv = mk_rsrc(ws + O_V, SZ_ACT);
    unsigned ob = (((rp >> 3) * 72 + (cp - 8) * 9) * 32 + (rp & 7) * 4 + wid) * BLK + lane * 16;
#pragma unroll
    for (int n = 0; n < NT; n++) st128(pack8(acc[0][n], acc[1][n]), rv, ob, n * 32 * BLK);
  }
#endif
}

__global__ __launch_bounds__(320) void k3_s(char* ws) {
#if defined(__HIP_DEVICE_COMPILE__)
  extern __shared__ __attribute__((aligned(16))) char shm[];
  constexpr int NT = 4;
  int bid = blockIdx.x, xcd = bid & 7, idx = bid >> 3;
  int rp = 2 * xcd + (idx & 1), kg = idx >> 1;
  int wid = __builtin_amdgcn_readfirstlane(threadIdx.x >> 6), lane = threadIdx.x & 63;
  if (wid == 4) { f_loader<NT, KD>(mk_rsrc(ws + O_H, SZ_ACT), ((rp >> 3) * 64 + kg * 4) * KD * BLK, shm, lane); return; }
  unsigned a0 = (rp * 8 + wid * 2) * KD * BLK + lane * 16, a1 = a0 + KD * BLK;
  f32x4 acc[2][NT]; zero(acc);
  f_consumer<NT, KD, 6, false, false>(mk_rsrc(ws + O_T, SZ_ACT), a0, a1, shm, lane, acc, 0);
  rsrc_t rpp = mk_rsrc(ws + O_P, 128 * 32 * BLK), rs = mk_rsrc(ws + O_ST, 128 * 16 * 128);
#pragma unroll
  for (int t = 0; t < 2; t++) {
    float m = acc[t][0][0];
#pragma unroll
    for (int n = 0; n < NT; n++)
#pragma unroll
      for (int j = 0; j < 4; j++) m = fmaxf(m, acc[t][n][j]);
    m = ceilf(qmax(m));
    float l = 0;
#pragma unroll
    for (int n = 0; n < NT; n++)
#pragma unroll
      for (int j = 0; j < 4; j++) { float p = __builtin_amdgcn_exp2f(acc[t][n][j] - m); acc[t][n][j] = p; l += p; }
    l = qsum(l);
    int rt = rp * 8 + wid * 2 + t;
    unsigned o = (rt * 32 + kg * 2) * BLK;
    st128(pack8(acc[t][0], acc[t][1]), rpp, lane * 16, o);
    st128(pack8(acc[t][2], acc[t][3]), rpp, lane * 16 + 1024, o);
    st64(u32x2{__float_as_uint(m), __float_as_uint(l)}, rs, lane < 16 ? lane * 8 : OOB, (rt * 16 + kg) * 128);
  }
#endif
}

constexpr int K4_LDS = 4 * 28 * 1024;
__global__ __launch_bounds__(384) void k4_o(const char* x, const char* wqc, const char* woc, char* ws) {
#if defined(__HIP_DEVICE_COMPILE__)
  extern __shared__ __attribute__((aligned(16))) char shm[];
  constexpr int NT = 6;
  int bid = blockIdx.x, xcd = bid & 7, idx = bid >> 3;
  int wid = __builtin_amdgcn_readfirstlane(threadIdx.x >> 6), lane = threadIdx.x & 63;
  if (idx >= 24) {
    int o = (idx - 24) * 8 + xcd;
    if (o < 18) tok_gemm<1>(ws + O_KC, wqc, ws, o, shm, wid, lane, 4);
    else if (o < 36) tok_gemm<2>(ws + O_VC, woc, ws, o - 18, shm, wid, lane, 4);
    return;
  }
  if (wid == 5) return;
  int rp = 2 * xcd + (idx & 1), dp = idx >> 1, b = rp >> 3;
  if (wid == 4) { f_loader<NT, 32>(mk_rsrc(ws + O_V, SZ_ACT), (b * 72 + dp * 6) * 32 * BLK, shm, lane); return; }
  int r = lane & 15, q = lane >> 4, rt0 = rp * 8 + wid * 2;
  rsrc_t rs = mk_rsrc(ws + O_ST, 128 * 16 * 128);
  u32x2 sv[2][4];
#pragma unroll
  for (int t = 0; t < 2; t++)
#pragma unroll
    for (int j = 0; j < 4; j++) sv[t][j] = ld64(rs, (q * 4 + j) * 128 + r * 8, (rt0 + t) * 2048);
  rsrc_t rx = mk_rsrc(x, 2048 * 2304), rm = mk_rsrc(ws + O_MOD, 13824);
  u32x2 xv[2][NT], gv[NT];
#pragma unroll
  for (int n = 0; n < NT; n++) {
    gv[n] = ld64(rm, b * 6912 + 4608 + dp * 192 + q * 8, n * 32);
#pragma unroll
    for (int t = 0; t < 2; t++) xv[t][n] = ld64(rx, ((rt0 + t) * 16 + r) * 2304 + dp * 192 + q * 8, n * 32);
  }
  LDS unsigned* tab = (LDS unsigned*)(shm + FR * NT * 2048 + wid * 2048);
  float inv[2];
#pragma unroll
  for (int t = 0; t < 2; t++) {
    float mg = fmaxf(fmaxf(__uint_as_float(sv[t][0][0]), __uint_as_float(sv[t][1][0])), fmaxf(__uint_as_float(sv[t][2][0]), __uint_as_float(sv[t][3][0])));
    mg = qmax(mg);
    float l = 0;
#pragma unroll
    for (int j = 0; j < 4; j++) {
      float d = fminf(mg - __uint_as_float(sv[t][j][0]), 255.f);
      l += __uint_as_float(sv[t][j][1]) * __builtin_amdgcn_exp2f(-d);
      tab[t * 256 + (q * 4 + j) * 16 + r] = (unsigned)d * 0x00800080u;
    }
    inv[t] = 1.f / qsum(l);
  }
  wait_lgkm<0>();
  unsigned a0 = rt0 * 32 * BLK + lane * 16, a1 = a0 + 32 * BLK;
  f32x4 acc[2][NT]; zero(acc);
  f_consumer<NT, 32, 8, false, true>(mk_rsrc(ws + O_P, 128 * 32 * BLK), a0, a1, shm, lane, acc, (unsigned)(size_t)tab + r * 4);
  rsrc_t ro = mk_rsrc(ws + O_X1, SZ_ACT);
#pragma unroll
  for (int t = 0; t < 2; t++) {
#pragma unroll
    for (int n = 0; n < NT; n++) {
      acc[t][n][0] = blo(xv[t][n][0]) + blo(gv[n][0]) * (acc[t][n][0] * inv[t]);
      acc[t][n][1] = bhi(xv[t][n][0]) + bhi(gv[n][0]) * (acc[t][n][1] * inv[t]);
      acc[t][n][2] = blo(xv[t][n][1]) + blo(gv[n][1]) * (acc[t][n][2] * inv[t]);
      acc[t][n][3] = bhi(xv[t][n][1]) + bhi(gv[n][1]) * (acc[t][n][3] * inv[t]);
    }
#pragma unroll
    for (int p = 0; p < 3; p++) st128(pack8(acc[t][2 * p], acc[t][2 * p + 1]), ro, lane * 16 + p * BLK, ((rt0 + t) * KD + dp * 3) * BLK);
  }
#endif
}

__global__ __launch_bounds__(256) void k5_x(char* ws, char* out) {
#if defined(__HIP_DEVICE_COMPILE__)
  __shared__ __attribute__((aligned(16))) float red[4 * 5 * 256 + 128];
  int bid = blockIdx.x, xcd = bid & 7, idx = bid >> 3;
  int rt = xcd * 16 + (idx >> 1), hf = idx & 1, b = rt >> 6;
  int wid = __builtin_amdgcn_readfirstlane(threadIdx.x >> 6), lane = threadIdx.x & 63, r = lane & 15, q = lane >> 4;
  rsrc_t rx = mk_rsrc(ws + O_X1, SZ_ACT), rw = mk_rsrc(ws + O_WQC, SZ_TOK), rv = mk_rsrc(ws + O_VOC, SZ_TOK);
  u32x4 xf[9], wf[9][5], vf[9][2]; u32x2 vh[9], xr[9];
#pragma unroll
  for (int s = 0; s < 9; s++) xf[s] = ld128(rx, lane * 16 + s * BLK, (rt * KD + wid * 9) * BLK);
#pragma unroll
  for (int s = 0; s < 9; s++)
#pragma unroll
    for (int t = 0; t < 5; t++) wf[s][t] = ld128(rw, lane * 16 + s * BLK, ((b * 5 + t) * KD + wid * 9) * BLK);
  int dt0 = hf * 36 + wid * 9;
#pragma unroll
  for (int n = 0; n < 9; n++) {
    unsigned o = (b * 72 + dt0 + n) * 2560;
    vf[n][0] = ld128(rv, lane * 16, o); vf[n][1] = ld128(rv, lane * 16 + 1024, o); vh[n] = ld64(rv, lane * 8 + 2048, o);
    xr[n] = ld64(rx, lane * 16 + ((dt0 + n) & 1) * 8, (rt * KD + ((dt0 + n) >> 1)) * BLK);
  }
  float* st = red + 4 * 5 * 256;
  float s1 = 0;
#pragma unroll
  for (int s = 0; s < 9; s++)
#pragma unroll
    for (int j = 0; j < 4; j++) s1 += blo(xf[s][j]) + bhi(xf[s][j]);
  s1 = qsum(s1);
  if (q == 0) st[wid * 16 + r] = s1;
  __syncthreads();
  float mean = (st[r] + st[16 + r] + st[32 + r] + st[48 + r]) * (1.f / DM), s2 = 0;
#pragma unroll
  for (int s = 0; s < 9; s++)
#pragma unroll
    for (int j = 0; j < 4; j++) { float a = blo(xf[s][j]) - mean, c = bhi(xf[s][j]) - mean; s2 += a * a + c * c; }
  s2 = qsum(s2);
  if (q == 0) st[64 + wid * 16 + r] = s2;
  __syncthreads();
  float rstd = rsqrtf((st[64 + r] + st[80 + r] + st[96 + r] + st[112 + r]) * (1.f / DM) + 1e-5f);
  f32x4 sc[5] = {};
#pragma unroll
  for (int s = 0; s < 9; s++) {
    u32x4 hc;
#pragma unroll
    for (int j = 0; j < 4; j++) hc[j] = pk2((blo(xf[s][j]) - mean) * rstd, (bhi(xf[s][j]) - mean) * rstd);
#pragma unroll
    for (int t = 0; t < 5; t++) sc[t] = __builtin_amdgcn_mfma_f32_16x16x32_bf16(__builtin_bit_cast(bf16x8, wf[s][t]), __builtin_bit_cast(bf16x8, hc), sc[t], 0, 0, 0);
  }
#pragma unroll
  for (int t = 0; t < 5; t++) *(f32x4*)(red + (wid * 5 + t) * 256 + lane * 4) = sc[t];
  __syncthreads();
  float m = -3.0e38f;
#pragma unroll
  for (int t = 0; t < 5; t++) {
    sc[t] = *(f32x4*)(red + t * 256 + lane * 4) + *(f32x4*)(red + (5 + t) * 256 + lane * 4) + *(f32x4*)(red + (10 + t) * 256 + lane * 4) + *(f32x4*)(red + (15 + t) * 256 + lane * 4);
  }
  bool pad = q == 3;
#pragma unroll
  for (int j = 1; j < 4; j++) sc[4][j] = pad ? -__builtin_inff() : sc[4][j];
#pragma unroll
  for (int t = 0; t < 5; t++)
#pragma unroll
    for (int j = 0; j < 4; j++) m = fmaxf(m, sc[t][j]);
  m = qmax(m);
  float l = 0;
#pragma unroll
  for (int t = 0; t < 5; t++)
#pragma unroll
    for (int j = 0; j < 4; j++) { float p = __builtin_amdgcn_exp2f(sc[t][j] - m); sc[t][j] = p; l += p; }
  float inv = 1.f / qsum(l);
  bf16x8 p0 = __builtin_bit_cast(bf16x8, pack8(sc[0] * inv, sc[1] * inv)), p1 = __builtin_bit_cast(bf16x8, pack8(sc[2] * inv, sc[3] * inv));
  u32x2 p2h = pack4(sc[4] * inv);
  bf16x8 p2 = __builtin_bit_cast(bf16x8, u32x4{p2h[0], p2h[1], 0u, 0u});
  rsrc_t ro = mk_rsrc(out, 2048 * 2304);
#pragma unroll
  for (int n = 0; n < 9; n++) {
    f32x4 o = {blo(xr[n][0]), bhi(xr[n][0]), blo(xr[n][1]), bhi(xr[n][1])};
    o = __builtin_amdgcn_mfma_f32_16x16x32_bf16(__builtin_bit_cast(bf16x8, vf[n][0]), p0, o, 0, 0, 0);
    o = __builtin_amdgcn_mfma_f32_16x16x32_bf16(__builtin_bit_cast(bf16x8, vf[n][1]), p1, o, 0, 0, 0);
    o = __builtin_amdgcn_mfma_f32_16x16x32_bf16(__builtin_bit_cast(bf16x8, u32x4{vh[n][0], vh[n][1], 0u, 0u}), p2, o, 0, 0, 0);
    st64(pack4(o), ro, (rt * 16 + r) * 2304 + q * 8, (dt0 + n) * 32);
  }
#endif
}

extern "C" void kernel_launch(void* const* d_in, const int* in_sizes, int n_in, void* d_out, int out_size, void* d_ws, size_t ws_size,
                              hipStream_t stream) {
  const char *x = (const char*)d_in[0], *c = (const char*)d_in[1], *y = (const char*)d_in[2], *wqkv = (const char*)d_in[3], *wo = (const char*)d_in[4],
             *wqc = (const char*)d_in[5], *wkvc = (const char*)d_in[6], *woc = (const char*)d_in[7], *wmod = (const char*)d_in[8];
  char* ws = (char*)d_ws;
  (void)hipFuncSetAttribute((const void*)k0_pre, hipFuncAttributeMaxDynamicSharedMemorySize, K0_LDS);
  (void)hipFuncSetAttribute((const void*)k2_tv, hipFuncAttributeMaxDynamicSharedMemorySize, 5 * 9 * 2048);
  (void)hipFuncSetAttribute((const void*)k4_o, hipFuncAttributeMaxDynamicSharedMemorySize, K4_LDS);
  k0_pre<<<256, 384, K0_LDS, stream>>>(x, c, y, wqkv, wo, wkvc, wmod, ws);
  k2_tv<<<256, 320, 5 * 9 * 2048, stream>>>(ws);
  k3_s<<<256, 320, 5 * 4 * 2048, stream>>>(ws);
  k4_o<<<256, 384, K4_LDS, stream>>>(x, wqc, woc, ws);
  k5_x<<<256, 256, 0, stream>>>(ws, (char*)d_out);
}
```

```cpp
#include <hip/hip_runtime.h>
#include <hip/hip_bf16.h>
#include <type_traits>

typedef __attribute__((ext_vector_type(8))) __bf16 bf16x8;
typedef __attribute__((ext_vector_type(2))) __bf16 bf16x2;
typedef __attribute__((ext_vector_type(4))) float f32x4;
typedef __attribute__((ext_vector_type(2))) float f32x2;
typedef __attribute__((ext_vector_type(4))) unsigned u32x4;
typedef __attribute__((ext_vector_type(3))) unsigned u32x3;
typedef __attribute__((ext_vector_type(2))) unsigned u32x2;
typedef unsigned long long u64;
#define LDS __attribute__((address_space(3)))
#define GAS __attribute__((address_space(1)))
#define DI __device__ __forceinline__

constexpr int DM = 1152;
constexpr int KD = 36;
constexpr int BLK = 1024;
constexpr size_t O_FLAG = 0;
constexpr size_t O_MOD = 4096;
constexpr size_t O_FL = 32768;
constexpr int FL_T = 0, FL_V = 128, FL_P = 256, FL_X = 512, FL_W = 768, FL_N = 832;
constexpr size_t O_H = 32768 + 131072;
constexpr size_t SZ_ACT = 128ull * 36 * 1024;
constexpr size_t O_W = O_H + SZ_ACT;
constexpr size_t O_T = O_W + 144ull * 36 * 1024;
constexpr size_t O_V = O_T + SZ_ACT;
constexpr size_t O_P = O_V + SZ_ACT;
constexpr size_t O_ST = O_P + 128ull * 32 * 1024;
constexpr size_t O_X1 = O_ST + 128ull * 16 * 16 * 8;
constexpr size_t SZ_TOK = 10ull * 36 * 1024;
constexpr size_t O_KC = O_X1 + SZ_ACT;
constexpr size_t O_VC = O_KC + SZ_TOK;
constexpr size_t O_WQC = O_VC + SZ_TOK;
constexpr size_t O_VOC = O_WQC + SZ_TOK;

constexpr size_t O_PROF = 200ull << 20;
#define STAMP(k, i)
#define ENDSTAMP(k, i)
#define K0AUX 16
#define K0PH 3

constexpr float QK_SCALE = 1.4426950408889634f * 0.029462782549439476f;

#if defined(__HIP_DEVICE_COMPILE__)
typedef __amdgpu_buffer_rsrc_t rsrc_t;
DI rsrc_t mk_rsrc(const void* p, unsigned bytes) { return __builtin_amdgcn_make_buffer_rsrc((void*)p, 0, bytes, 0x00020000); }
DI u32x4 mk_desc(const void* p, unsigned bytes) { u64 a = (u64)p; return u32x4{(unsigned)a, (unsigned)(a >> 32) & 0xffffu, bytes, 0x00020000u}; }
DI unsigned pk2(float a, float b) { f32x2 p = {a, b}; return __builtin_bit_cast(unsigned, __builtin_convertvector(p, bf16x2)); }
DI float blo(unsigned u) { return __uint_as_float(u << 16); }
DI float bhi(unsigned u) { return __uint_as_float(u & 0xffff0000u); }
template <int N> DI void wait_vm() { asm volatile("s_waitcnt vmcnt(%0)" ::"n"(N > 63 ? 63 : N) : "memory"); }
template <int N> DI void wait_lgkm() { asm volatile("s_waitcnt lgkmcnt(%0)" ::"n"(N > 15 ? 15 : N) : "memory"); }
DI void SB() { __builtin_amdgcn_sched_barrier(0); }
DI void bar() { asm volatile("s_barrier" ::: "memory"); }
template <int I, int N, class F> DI void sfor(F&& f) { if constexpr (I < N) { f(std::integral_constant<int, I>{}); sfor<I + 1, N>(f); } }
template <int V> using ic = std::integral_constant<int, V>;
DI void mfma(f32x4& c, const u32x4& x, const u32x4& y) { asm volatile("v_mfma_f32_16x16x32_bf16 %0, %1, %2, %0" : "+v"(c) : "v"(x), "v"(y)); }
DI f32x4 mfma_b(const u32x4& x, const u32x4& y, const f32x4& c) {
  return __builtin_amdgcn_mfma_f32_16x16x32_bf16(__builtin_bit_cast(bf16x8, x), __builtin_bit_cast(bf16x8, y), c, 0, 0, 0);
}
template <int OFF> DI void dsr128(u32x4& d, unsigned a) { asm volatile("ds_read_b128 %0, %1 offset:%2" : "=v"(d) : "v"(a), "n"(OFF)); }
template <int OFF> DI void dsr_tr(u32x2& d, unsigned a) { asm volatile("ds_read_b64_tr_b16 %0, %1 offset:%2" : "=v"(d) : "v"(a), "n"(OFF)); }
constexpr int SC1 = 16;
template <int AUX = 0> DI u32x4 ld128(rsrc_t r, unsigned v, unsigned s) { return __builtin_amdgcn_raw_buffer_load_b128(r, v, s, AUX); }
template <int AUX = 0> DI u32x2 ld64(rsrc_t r, unsigned v, unsigned s) { return __builtin_amdgcn_raw_buffer_load_b64(r, v, s, AUX); }
template <int AUX = 0> DI void st128(u32x4 d, rsrc_t r, unsigned v, unsigned s) {
  __builtin_amdgcn_raw_buffer_store_b128(d, r, v, s, AUX); SB(); asm volatile("s_nop 1"); SB();
}
template <int AUX = 0> DI void st64(u32x2 d, rsrc_t r, unsigned v, unsigned s) { __builtin_amdgcn_raw_buffer_store_b64(d, r, v, s, AUX); }
template <int IMM, int AUX = 0> DI void dma16(rsrc_t r, LDS char* d, unsigned v, unsigned s) { __builtin_amdgcn_raw_ptr_buffer_load_lds(r, d, 16, v, s, IMM, AUX); }
DI void dma4_asm(u32x4 desc, unsigned lds, unsigned v0, unsigned v1, unsigned v2, unsigned v3, unsigned s) {
  unsigned keep;
  asm volatile("s_mov_b32 %0, m0\n\ts_mov_b32 m0, %2\n\ts_nop 0\n\t"
               "buffer_load_dwordx4 %3, %1, %7 offen lds\n\tbuffer_load_dwordx4 %4, %1, %7 offen offset:1024 lds\n\t"
               "buffer_load_dwordx4 %5, %1, %7 offen offset:2048 lds\n\tbuffer_load_dwordx4 %6, %1, %7 offen offset:3072 lds\n\ts_mov_b32 m0, %0"
               : "=&s"(keep) : "s"(desc), "s"(lds), "v"(v0), "v"(v1), "v"(v2), "v"(v3), "s"(s) : "memory");
}
DI u32x4 pack8(const f32x4& a, const f32x4& b) { return u32x4{pk2(a[0], a[1]), pk2(a[2], a[3]), pk2(b[0], b[1]), pk2(b[2], b[3])}; }
DI u32x2 pack4(const f32x4& a) { return u32x2{pk2(a[0], a[1]), pk2(a[2], a[3])}; }
DI void mfma_done() { asm volatile("s_nop 15" ::: "memory"); }
template <class T> DI void pin(T& x) { asm volatile("" : "+v"(x)); }
DI float qmax(float x) {
  auto a = __builtin_amdgcn_permlane32_swap(__float_as_uint(x), __float_as_uint(x), false, false);
  x = fmaxf(__uint_as_float(a[0]), __uint_as_float(a[1]));
  auto b = __builtin_amdgcn_permlane16_swap(__float_as_uint(x), __float_as_uint(x), false, false);
  return fmaxf(__uint_as_float(b[0]), __uint_as_float(b[1]));
}
DI float qsum(float x) {
  auto a = __builtin_amdgcn_permlane32_swap(__float_as_uint(x), __float_as_uint(x), false, false);
  x = __uint_as_float(a[0]) + __uint_as_float(a[1]);
  auto b = __builtin_amdgcn_permlane16_swap(__float_as_uint(x), __float_as_uint(x), false, false);
  return __uint_as_float(b[0]) + __uint_as_float(b[1]);
}
constexpr unsigned OOB = 0x7ff00000u;
constexpr int SPIN_CAP = 1 << 18;
template <int NX, int NY> DI void zero(f32x4 (&a)[NX][NY]) {
#pragma unroll
  for (int i = 0; i < NX; i++)
#pragma unroll
    for (int j = 0; j < NY; j++) a[i][j] = f32x4{0, 0, 0, 0};
}
template <int NX, int NY> DI void pin2(f32x4 (&a)[NX][NY]) {
#pragma unroll
  for (int i = 0; i < NX; i++)
#pragma unroll
    for (int j = 0; j < NY; j++) pin(a[i][j]);
}

DI void publish(char* ws, unsigned cnt_lds, int nw, int flag, int lane) {
  wait_vm<0>();
  unsigned old, one = lane == 0;
  asm volatile("ds_add_rtn_u32 %0, %1, %2\n\ts_waitcnt lgkmcnt(0)" : "=&v"(old) : "v"(cnt_lds), "v"(one) : "memory");
  if (__builtin_amdgcn_readfirstlane(old) == nw - 1)
    __builtin_amdgcn_raw_buffer_store_b32(1u, mk_rsrc(ws + O_FL, FL_N * 128), lane == 0 ? 0u : OOB, flag * 128, SC1);
}
DI void poll(const char* ws, int flag_of_lane) {
  GAS const unsigned* p = (GAS const unsigned*)(ws + O_FL) + flag_of_lane * 32;
#pragma unroll 1
  for (int spin = 0; spin < SPIN_CAP; spin++) {
    unsigned v = __hip_atomic_load(p, __ATOMIC_RELAXED, __HIP_MEMORY_SCOPE_AGENT);
    if (__all(v == 1u)) break;
    __builtin_amdgcn_s_sleep(1);
  }
  asm volatile("" ::: "memory"); SB();
}

template <int NT, int KS, int D, bool PERG, int START, int WRAP, bool SWAP, bool SCALE, int AUX, class PRE>
DI void f_consumer(rsrc_t ra, unsigned a0, unsigned a1, unsigned lp, int pb, f32x4 (&acc)[2][NT], unsigned tab, PRE&& pre) {
  constexpr int U = D / 2, NB = KS / D, SLOT = NT * 2048, REG = U * SLOT;
  static_assert(KS % D == 0, "");
  u32x4 a[D][2];
  unsigned sa = pb * D * BLK;
#pragma unroll
  for (int d = 0; d < D - 1; d++) { a[d][0] = ld128<AUX>(ra, a0, sa + d * BLK); SB(); a[d][1] = ld128<AUX>(ra, a1, sa + d * BLK); SB(); }
  u32x4 b0[NT], b1[NT], sh[2], shn[2];
  auto scale = [&](u32x4& v, int j, unsigned s) { asm volatile("v_pk_sub_u16 %0, %0, %1 clamp" : "+v"(v[j]) : "v"(s)); };
  pre();
  if constexpr (SCALE) {
    dsr128<0>(sh[0], tab); dsr128<1024>(sh[1], tab);
    wait_lgkm<0>(); pin(sh[0]); pin(sh[1]);
#pragma unroll
    for (int j = 0; j < 4; j++) { scale(a[0][0], j, sh[0][0]); scale(a[0][1], j, sh[1][0]); }
  }
  auto half = [&](u32x4 (&av)[2], u32x4 (&bc)[NT], u32x4 (&bn)[NT], unsigned addr, auto off, u32x4 (&ar)[2], unsigned so, auto nx,
                  u32x4 (&an)[2], unsigned s0, unsigned s1) {
    sfor<0, NT>([&](auto i) {
      constexpr int I = decltype(i)::value, WS = NT == 4 ? 2 : 3;
      if constexpr (I % WS == 0) { constexpr int need = (I + WS < NT ? I + WS : NT); wait_lgkm<NT - need + I + decltype(nx)::value>(); }
      if constexpr (SWAP) mfma(acc[0][I], av[0], bc[I]); else mfma(acc[0][I], bc[I], av[0]);
      dsr128<I * 2048 + decltype(off)::value>(bn[I], addr);
      if constexpr (SWAP) mfma(acc[1][I], av[1], bc[I]); else mfma(acc[1][I], bc[I], av[1]);
      if constexpr (I == 0) { SB(); ar[0] = ld128<AUX>(ra, a0, so); SB(); }
      if constexpr (I == 2) { SB(); ar[1] = ld128<AUX>(ra, a1, so); SB(); }
      if constexpr (SCALE && I >= 1 && I <= 4) { scale(an[(I - 1) >> 1], ((I - 1) & 1) * 2, I < 3 ? s0 : s1); scale(an[(I - 1) >> 1], ((I - 1) & 1) * 2 + 1, I < 3 ? s0 : s1); }
    });
  };
  pin2(acc);
  SB();
  bar();
  unsigned cur = lp + START; int reg = START;
  sfor<0, NT>([&](auto i) { dsr128<decltype(i)::value * 2048>(b0[decltype(i)::value], cur); });
#pragma unroll 1
  for (int it = 0; it < NB; it++) {
    bool last = it == NB - 1;
    pb = pb + 1 == NB ? 0 : pb + 1;
    unsigned sn = last ? OOB : pb * D * BLK;
    reg = reg + REG == WRAP ? 0 : reg + REG;
    unsigned nxt = lp + reg;
    if constexpr (SCALE) { tab += 16; dsr128<0>(shn[0], tab); dsr128<1024>(shn[1], tab); }
    sfor<0, U>([&](auto uu) {
      constexpr int u = decltype(uu)::value;
      half(a[2 * u], b0, b1, cur, ic<u * SLOT + 1024>{}, a[(2 * u + D - 1) % D], u == 0 ? sa + (D - 1) * BLK : sn + (2 * u - 1) * BLK,
           ic<(SCALE && u == 0) ? 2 : 0>{}, a[2 * u + 1], sh[0][u & 3], sh[1][u & 3]);
      if constexpr (u < U - 1) {
        if constexpr (PERG) bar();
        half(a[2 * u + 1], b1, b0, cur, ic<(u + 1) * SLOT>{}, a[2 * u], sn + 2 * u * BLK, ic<0>{}, a[2 * u + 2], sh[0][(u + 1) & 3], sh[1][(u + 1) & 3]);
      } else {
        if (!last) bar();
        if constexpr (SCALE) { pin(shn[0]); pin(shn[1]); }
        half(a[2 * u + 1], b1, b0, nxt, ic<0>{}, a[2 * u], sn + 2 * u * BLK, ic<0>{}, a[0], shn[0][0], shn[1][0]);
      }
    });
    if constexpr (SCALE) { sh[0] = shn[0]; sh[1] = shn[1]; }
    cur = nxt; sa = sn;
  }
  wait_lgkm<0>();
  mfma_done();
  pin2(acc);
}
template <int NT, int KS, int AUX> struct FSrc {
  rsrc_t r; unsigned v[NT]; int rot;
  DI void init(rsrc_t rb, unsigned boff, int rot_, int lane) {
    r = rb; rot = rot_;
#pragma unroll
    for (int n = 0; n < NT; n++) v[n] = boff + lane * 16 + n * KS * BLK - (n & 1) * 2048;
  }
  DI void issue(int g, LDS char* dst) const {
    int gg = g + rot; gg = gg >= KS / 2 ? gg - KS / 2 : gg;
    unsigned so = gg * 2048;
    sfor<0, NT>([&](auto i) {
      constexpr int n = decltype(i)::value;
      dma16<(n & 1) * 2048, AUX>(r, dst + (n >> 1) * 4096, v[n], so);
      dma16<(n & 1) * 2048 + 1024, AUX>(r, dst + (n >> 1) * 4096, v[n], so);
    });
  }
};

enum { KC = 0, TR = 1, FL = 2 };
template <int NP> struct RSrc {
  rsrc_t r; unsigned v[NP]; unsigned kstep;
  DI void kc(unsigned base, unsigned stride, int lane) {
#pragma unroll
    for (int p = 0; p < NP; p++) { int row = 8 * p + (lane >> 3); v[p] = base + row * stride + (((lane & 7) ^ ((row >> 1) & 7)) << 4) - (p & 3) * 1024; }
    kstep = 128;
  }
  DI void tr(unsigned base, unsigned stride, int lane) {
#pragma unroll
    for (int p = 0; p < NP; p++) {
      int row = 8 * (p & 7) + (lane >> 3), f = ((row >> 1) & 1) | (((row >> 3) & 1) << 1);
      v[p] = base + (p >> 3) * 128 + row * stride + (((lane & 7) ^ (f << 1)) << 4) - (p & 3) * 1024;
    }
    kstep = 64 * stride;
  }
  DI void fl(unsigned base, int lane) {
#pragma unroll
    for (int p = 0; p < NP; p++) v[p] = base + (p >> 1) * KD * BLK + (p & 1) * BLK + lane * 16 - (p & 3) * 1024;
    kstep = 2048;
  }
  DI void issue(int gg, LDS char* dst) const {
    unsigned so = gg * kstep;
    sfor<0, NP>([&](auto i) { constexpr int p = decltype(i)::value; dma16<(p & 3) * 1024>(r, dst + (p >> 2) * 4096, v[p], so); });
  }
};
template <int NP, class F> DI void r_loader(F&& iss, int rot) {
  constexpr int G = 18;
  auto issue = [&](int g, int slot) { int gg = g + rot; iss(gg >= G ? gg - G : gg, slot); };
  issue(0, 0); issue(1, 1);
  wait_vm<NP>(); bar();
  issue(2, 2); issue(3, 3);
  int slot = 0;
#pragma unroll 1
  for (int g = 1; g < G - 3; g++) {
    wait_vm<2 * NP>(); bar();
    issue(g + 3, slot); slot = (slot + 1) & 3;
  }
  wait_vm<2 * NP>(); bar();
  wait_vm<NP>(); bar();
  wait_vm<0>(); bar();
}
template <int KIND, int NTL> struct Frag {
  unsigned base[KIND == TR ? NTL : (KIND == KC ? 2 : 1)];
  DI void init(unsigned lds0, int ioff, int t0, int lane) {
    int r = lane & 15, q = lane >> 4;
    if constexpr (KIND == KC) {
      unsigned b = lds0 + ioff + (t0 * 16 + r) * 128;
      base[0] = b + ((q ^ ((r >> 1) & 7)) << 4); base[1] = b + (((4 + q) ^ ((r >> 1) & 7)) << 4);
    } else if constexpr (KIND == TR) {
      int qq = r >> 2, p = r & 3, f = ((qq >> 1) & 1) | ((q & 1) << 1);
#pragma unroll
      for (int c = 0; c < NTL; c++) { int t = t0 + c; base[c] = lds0 + ioff + (t >> 2) * 8192 + (8 * q + qq) * 128 + (((t & 3) ^ f) << 5) + p * 8; }
    } else base[0] = lds0 + ioff + t0 * 2048 + lane * 16;
  }
  template <int H, int I> DI void read(u32x4& d, unsigned so) const {
    if constexpr (KIND == KC) dsr128<I * 2048>(d, base[H] + so);
    else if constexpr (KIND == FL) dsr128<I * 2048 + H * 1024>(d, base[0] + so);
    else {
      u32x2 lo, hi;
      dsr_tr<H * 4096>(lo, base[I] + so); dsr_tr<H * 4096 + 512>(hi, base[I] + so);
      d = u32x4{lo[0], lo[1], hi[0], hi[1]};
    }
  }
};
template <int KX, int NX, int KY, int NY>
DI void r_consumer(const Frag<KX, NX>& fx, const Frag<KY, NY>& fy, int slot_bytes, f32x4 (&acc)[NX][NY]) {
  constexpr int G = 18;
  u32x4 x0[NX], y0[NY], x1[NX], y1[NY];
  auto half = [&](u32x4 (&xc)[NX], u32x4 (&yc)[NY], u32x4 (&xn)[NX], u32x4 (&yn)[NY], unsigned so, auto hh) {
    constexpr int H = decltype(hh)::value;
    sfor<0, NX * NY>([&](auto i) {
      constexpr int I = decltype(i)::value;
      mfma(acc[I / NY][I % NY], xc[I / NY], yc[I % NY]);
      if constexpr (I < NY) fy.template read<H, I>(yn[I], so);
      else if constexpr (I < NX + NY) fx.template read<H, I - NY>(xn[I - NY], so);
    });
    wait_lgkm<0>();
#pragma unroll
    for (int i = 0; i < NX; i++) pin(xn[i]);
#pragma unroll
    for (int i = 0; i < NY; i++) pin(yn[i]);
  };
  pin2(acc);
  SB();
  bar();
  sfor<0, NY>([&](auto i) { fy.template read<0, decltype(i)::value>(y0[decltype(i)::value], 0); });
  sfor<0, NX>([&](auto i) { fx.template read<0, decltype(i)::value>(x0[decltype(i)::value], 0); });
  wait_lgkm<0>();
#pragma unroll
  for (int i = 0; i < NX; i++) pin(x0[i]);
#pragma unroll
  for (int i = 0; i < NY; i++) pin(y0[i]);
  int slot = 0; unsigned so = 0;
#pragma unroll 1
  for (int g = 0; g < G; g++) {
    half(x0, y0, x1, y1, so, ic<1>{});
    slot = (slot + 1) & 3; so = slot * slot_bytes;
    if (g < G - 1) bar();
    half(x1, y1, x0, y0, so, ic<0>{});
  }
  mfma_done();
  pin2(acc);
}
DI u64 mix64(u64 z) { z += 0x9E3779B97F4A7C15ull; z = (z ^ (z >> 30)) * 0xBF58476D1CE4E5B9ull; z = (z ^ (z >> 27)) * 0x94D049BB133111EBull; return z ^ (z >> 31); }
#endif

constexpr int L_TAB = 147456, L_CNT = L_TAB + 8192, L_ST = L_CNT + 64, KF_LDS = L_ST + 512;
extern "C" __global__ __launch_bounds__(320) void kf_main(const char* x, const char* wqc, const char* woc, char* ws, char* out) {
#if defined(__HIP_DEVICE_COMPILE__)
  extern __shared__ __attribute__((aligned(16))) char shm[];
  STAMP(1, 0);
  const int bid = blockIdx.x, xcd = bid & 7, idx = bid >> 3;
  const int wid = __builtin_amdgcn_readfirstlane(threadIdx.x >> 6), lane = threadIdx.x & 63, r = lane & 15, q = lane >> 4;
  LDS char* sm = (LDS char*)shm;
  const unsigned l0 = (unsigned)(size_t)sm;
  const int rp2 = 4 * (xcd & 3) + (idx & 3), cp = 8 * (xcd >> 2) + (idx >> 2), ph2 = ((idx ^ (idx >> 2)) & 1) * 3;
  const int rp = 2 * xcd + (idx & 1), kg = idx >> 1, b = rp >> 3, ph3 = kg % 3;
  const int dp = idx >> 1, ph4 = dp & 3;
  const int so = (idx - 24) * 8 + xcd;
  const int rt5 = xcd * 16 + (idx >> 1), hf = idx & 1, b5 = rt5 >> 6;
  constexpr int TSLOT = 28 * 1024, TW = 20 * 1024;

  if (wid == 4) {
    {
      FSrc<9, KD, 0> w; w.init(mk_rsrc(ws + O_W, 144 * KD * BLK), cp * 9 * KD * BLK, ph2 * 3, lane);
      FSrc<4, KD, 0> h; h.init(mk_rsrc(ws + O_H, SZ_ACT), (b * 64 + kg * 4) * KD * BLK, ph3 * 6, lane);
      w.issue(0, sm); w.issue(1, sm + 18432);
      wait_vm<18>(); bar();
      w.issue(2, sm + 2 * 18432); w.issue(3, sm + 3 * 18432);
      int slot = 4;
#pragma unroll 1
      for (int g = 1; g < 15; g++) {
        wait_vm<36>(); bar();
        w.issue(g + 3, sm + slot * 18432); slot = slot == 5 ? 0 : slot + 1;
      }
      wait_vm<36>(); bar();
      h.issue(2, sm + 98304 + 2 * 8192); h.issue(3, sm + 98304 + 3 * 8192);
      wait_vm<34>(); bar();
      h.issue(4, sm + 98304 + 4 * 8192); h.issue(5, sm + 98304 + 5 * 8192);
      wait_vm<32>(); bar();
      bar();
      h.issue(0, sm + 98304); h.issue(1, sm + 98304 + 8192);
#pragma unroll 1
      for (int g = 6; g < 18; g++) h.issue(g, sm + (g - 6) * 8192);
      wait_vm<63>(); bar();
      wait_vm<48>(); bar();
    }
    if (idx < 24) {
      poll(ws, FL_V + (lane & 8 ? (dp * 6 + 5) / 9 : (dp * 6) / 9) * 16 + b * 8 + (lane & 7));
      __builtin_amdgcn_fence(__ATOMIC_ACQUIRE, "agent");
      FSrc<6, 32, SC1> v; v.init(mk_rsrc(ws + O_V, SZ_ACT), (b * 72 + dp * 6) * 32 * BLK, ph4 * 4, lane);
      wait_vm<0>(); bar();
#pragma unroll 1
      for (int g = 0; g < 7; g++) v.issue(g, sm + (g < 4 ? 98304 + g * 12288 : (g - 4) * 12288));
      bar();
#pragma unroll 1
      for (int g = 7; g < 12; g++) v.issue(g, sm + (g - 4) * 12288);
      wait_vm<63>(); bar();
      wait_vm<48>(); bar();
      wait_vm<0>(); bar();
#pragma unroll 1
      for (int g = 12; g < 16; g++) v.issue(g, sm + 98304 + (g - 12) * 12288);
      wait_vm<0>(); bar();
      bar();
    } else {
      wait_vm<0>(); bar();
      bar();
      if (so < 36) {
        RSrc<10> t; t.r = mk_rsrc(ws + O_KC, SZ_TOK); t.fl((so & 1) * 5 * KD * BLK, lane);
        RSrc<8> w; w.r = mk_rsrc(wqc, DM * 2304); w.kc((so >> 1) * 64 * 2304, 2304, lane);
        r_loader<18>([&](int g, int slot) { t.issue(g, sm + slot * TSLOT); w.issue(g, sm + slot * TSLOT + TW); }, 0);
        bar();
      } else if (so < 54) {
        RSrc<20> t; t.r = mk_rsrc(ws + O_VC, SZ_TOK); t.fl(0, lane);
        RSrc<8> w; w.r = mk_rsrc(woc, DM * 2304); w.tr((so - 36) * 128, 2304, lane);
        r_loader<28>([&](int g, int slot) { t.issue(g, sm + slot * TSLOT); w.issue(g, sm + slot * TSLOT + TW); }, 0);
        bar();
      }
    }
    poll(ws, FL_W + 2 * (lane < 18 ? lane : 0) + b5);
    __builtin_amdgcn_fence(__ATOMIC_ACQUIRE, "agent");
    {
      rsrc_t rw = mk_rsrc(ws + O_WQC, SZ_TOK);
      unsigned v0 = b5 * 5 * KD * BLK + lane * 16, v1 = v0 + KD * BLK - 1024, v2 = v0 + 2 * KD * BLK - 2048, v3 = v0 + 3 * KD * BLK - 3072;
#pragma unroll 1
      for (int s = 0; s < KD; s++) {
        dma16<0, SC1>(rw, sm + s * 4096, v0, s * BLK); dma16<1024, SC1>(rw, sm + s * 4096, v1, s * BLK);
        dma16<2048, SC1>(rw, sm + s * 4096, v2, s * BLK); dma16<3072, SC1>(rw, sm + s * 4096, v3, s * BLK);
      }
    }
    wait_vm<0>(); bar();
    return;
  }

  const unsigned lp = l0 + lane * 16;
  if (wid == 0) *(LDS u32x4*)(sm + L_CNT) = u32x4{0, 0, 0, 0};
  {
    rsrc_t ra = mk_rsrc(ws + O_H, SZ_ACT);
    unsigned a0 = (rp2 * 8 + wid * 2) * KD * BLK + lane * 16, a1 = a0 + KD * BLK;
    f32x4 acc[2][9]; zero(acc);
    if (cp < 8) {
      asm volatile("; T" : "+v"(a0), "+v"(a1));
      f_consumer<9, KD, 6, true, 0, 110592, false, false, 0>(ra, a0, a1, lp, ph2, acc, 0, [&] { wait_lgkm<0>(); });
      STAMP(1, 1);
      rsrc_t rt = mk_rsrc(ws + O_T, SZ_ACT);
      unsigned ob = (rp2 * 8 + wid * 2) * KD * BLK + lane * 16 + ((cp * 9) >> 1) * BLK;
      if (cp & 1) {
#pragma unroll
        for (int t = 0; t < 2; t++) {
          st64<SC1>(pack4(acc[t][0]), rt, ob + 8, t * KD * BLK);
#pragma unroll
          for (int p = 0; p < 4; p++) st128<SC1>(pack8(acc[t][1 + 2 * p], acc[t][2 + 2 * p]), rt, ob + (p + 1) * BLK, t * KD * BLK);
        }
      } else {
#pragma unroll
        for (int t = 0; t < 2; t++) {
#pragma unroll
          for (int p = 0; p < 4; p++) st128<SC1>(pack8(acc[t][2 * p], acc[t][2 * p + 1]), rt, ob + p * BLK, t * KD * BLK);
          st64<SC1>(pack4(acc[t][8]), rt, ob + 4 * BLK, t * KD * BLK);
        }
      }
    } else {
      asm volatile("; V" : "+v"(a0), "+v"(a1));
      f_consumer<9, KD, 6, true, 0, 110592, true, false, 0>(ra, a0, a1, lp, ph2, acc, 0, [&] { wait_lgkm<0>(); });
      STAMP(1, 1);
      rsrc_t rv = mk_rsrc(ws + O_V, SZ_ACT);
      unsigned ob = (((rp2 >> 3) * 72 + (cp - 8) * 9) * 32 + (rp2 & 7) * 4 + wid) * BLK + lane * 16;
#pragma unroll
      for (int n = 0; n < 9; n++) st128<SC1>(pack8(acc[0][n], acc[1][n]), rv, ob, n * 32 * BLK);
    }
    SB(); bar();
    publish(ws, l0 + L_CNT, 4, cp < 8 ? FL_T + rp2 * 8 + cp : FL_V + (cp - 8) * 16 + rp2, lane);
    STAMP(1, 2);
  }
  const int rt0 = rp * 8 + wid * 2;
  {
    f32x4 acc[2][4]; zero(acc);
    poll(ws, FL_T + rp * 8 + (lane & 7));
    STAMP(1, 3);
    unsigned a0 = rt0 * KD * BLK + lane * 16, a1 = a0 + KD * BLK;
    f_consumer<4, KD, 12, false, 98304, 147456, false, false, SC1>(mk_rsrc(ws + O_T, SZ_ACT), a0, a1, lp, ph3, acc, 0, [] {});
    STAMP(1, 4);
    rsrc_t rpp = mk_rsrc(ws + O_P, 128 * 32 * BLK), rs = mk_rsrc(ws + O_ST, 128 * 16 * 128);
#pragma unroll
    for (int t = 0; t < 2; t++) {
      float m = acc[t][0][0];
#pragma unroll
      for (int n = 0; n < 4; n++)
#pragma unroll
        for (int j = 0; j < 4; j++) m = fmaxf(m, acc[t][n][j]);
      m = ceilf(qmax(m));
      float l = 0;
#pragma unroll
      for (int n = 0; n < 4; n++)
#pragma unroll
        for (int j = 0; j < 4; j++) { float p = __builtin_amdgcn_exp2f(acc[t][n][j] - m); acc[t][n][j] = p; l += p; }
      unsigned o = ((rt0 + t) * 32 + kg * 2) * BLK;
      st128<SC1>(pack8(acc[t][0], acc[t][1]), rpp, lane * 16, o);
      st128<SC1>(pack8(acc[t][2], acc[t][3]), rpp, lane * 16 + 1024, o);
      l = qsum(l);
      st64<SC1>(u32x2{__float_as_uint(m), __float_as_uint(l)}, rs, lane < 16 ? lane * 8 : OOB, ((rt0 + t) * 16 + kg) * 128);
    }
    SB(); bar();
    publish(ws, l0 + L_CNT + 4, 4, FL_P + rp * 16 + kg, lane);
    STAMP(1, 5);
  }
  if (idx < 24) {
    rsrc_t rx = mk_rsrc(x, 2048 * 2304), rm = mk_rsrc(ws + O_MOD, 13824), rs = mk_rsrc(ws + O_ST, 128 * 16 * 128);
    u32x2 xv[2][6], gv[6], sv[2][4];
#pragma unroll
    for (int n = 0; n < 6; n++) {
      gv[n] = ld64(rm, b * 6912 + 4608 + dp * 192 + q * 8, n * 32);
#pragma unroll
      for (int t = 0; t < 2; t++) xv[t][n] = ld64(rx, ((rt0 + t) * 16 + r) * 2304 + dp * 192 + q * 8, n * 32);
    }
    f32x4 acc[2][6]; zero(acc);
    poll(ws, FL_P + rp * 16 + r);
    STAMP(1, 6);
#pragma unroll
    for (int t = 0; t < 2; t++)
#pragma unroll
      for (int j = 0; j < 4; j++) sv[t][j] = ld64<SC1>(rs, (q * 4 + j) * 128 + r * 8, (rt0 + t) * 2048);
    SB();
    LDS unsigned* tab = (LDS unsigned*)(sm + L_TAB + wid * 2048);
    float inv[2];
    unsigned a0 = rt0 * 32 * BLK + lane * 16, a1 = a0 + 32 * BLK;
    f_consumer<6, 32, 8, false, 98304, 147456, false, true, SC1>(mk_rsrc(ws + O_P, 128 * 32 * BLK), a0, a1, lp, ph4, acc, l0 + L_TAB + wid * 2048 + r * 64, [&] {
#pragma unroll
      for (int t = 0; t < 2; t++) {
        float mg = qmax(fmaxf(fmaxf(__uint_as_float(sv[t][0][0]), __uint_as_float(sv[t][1][0])), fmaxf(__uint_as_float(sv[t][2][0]), __uint_as_float(sv[t][3][0]))));
        float l = 0; u32x4 k;
#pragma unroll
        for (int j = 0; j < 4; j++) {
          float d = fminf(mg - __uint_as_float(sv[t][j][0]), 255.f);
          l += __uint_as_float(sv[t][j][1]) * __builtin_amdgcn_exp2f(-d);
          k[j] = (unsigned)d * 0x00800080u;
        }
        *(LDS u32x4*)(tab + t * 256 + r * 16 + ((q - ph4) & 3) * 4) = k;
        inv[t] = __builtin_amdgcn_rcpf(qsum(l));
      }
      wait_lgkm<0>();
    });
    STAMP(1, 7);
    rsrc_t ro = mk_rsrc(ws + O_X1, SZ_ACT);
#pragma unroll
    for (int t = 0; t < 2; t++) {
#pragma unroll
      for (int n = 0; n < 6; n++) {
        acc[t][n][0] = blo(xv[t][n][0]) + blo(gv[n][0]) * (acc[t][n][0] * inv[t]);
        acc[t][n][1] = bhi(xv[t][n][0]) + bhi(gv[n][0]) * (acc[t][n][1] * inv[t]);
        acc[t][n][2] = blo(xv[t][n][1]) + blo(gv[n][1]) * (acc[t][n][2] * inv[t]);
        acc[t][n][3] = bhi(xv[t][n][1]) + bhi(gv[n][1]) * (acc[t][n][3] * inv[t]);
      }
#pragma unroll
      for (int p = 0; p < 3; p++) st128<SC1>(pack8(acc[t][2 * p], acc[t][2 * p + 1]), ro, lane * 16 + p * BLK, ((rt0 + t) * KD + dp * 3) * BLK);
    }
    SB(); bar();
    publish(ws, l0 + L_CNT + 8, 4, FL_X + rp * 16 + dp, lane);
  } else if (so < 36) {
    if (wid < 2) {
      Frag<FL, 5> ft; ft.init(l0, 0, 0, lane);
      Frag<KC, 2> fw; fw.init(l0, TW, wid * 2, lane);
      f32x4 acc[2][5]; zero(acc);
      r_consumer(fw, ft, TSLOT, acc);
      STAMP(1, 7);
      rsrc_t ro = mk_rsrc(ws + O_WQC, SZ_TOK);
#pragma unroll
      for (int t = 0; t < 5; t++) st128<SC1>(pack8(acc[0][t] * QK_SCALE, acc[1][t] * QK_SCALE), ro, lane * 16, (((so & 1) * 5 + t) * KD + (so >> 1) * 2 + wid) * BLK);
      SB(); bar();
      publish(ws, l0 + L_CNT + 8, 2, FL_W + so, lane);
    } else {
#pragma unroll 1
      for (int g = 0; g < 19; g++) bar();
    }
  } else if (so < 54) {
    int bb = wid >> 1, wn = wid & 1;
    Frag<FL, 5> ft; ft.init(l0, 0, bb * 5, lane);
    Frag<TR, 2> fw; fw.init(l0, TW, wn * 2, lane);
    f32x4 acc[5][2]; zero(acc);
    r_consumer(ft, fw, TSLOT, acc);
    STAMP(1, 7);
    rsrc_t ro = mk_rsrc(ws + O_VOC, SZ_TOK);
#pragma unroll
    for (int j = 0; j < 2; j++) {
      unsigned o = (bb * 72 + (so - 36) * 4 + wn * 2 + j) * 2560;
      st128<SC1>(pack8(acc[0][j], acc[1][j]), ro, lane * 16, o);
      st128<SC1>(pack8(acc[2][j], acc[3][j]), ro, lane * 16 + 1024, o);
      st64<SC1>(pack4(acc[4][j]), ro, lane * 8 + 2048, o);
    }
    SB(); bar();
    publish(ws, l0 + L_CNT + 8, 4, FL_W + so, lane);
  }
  STAMP(1, 8);
  {
    rsrc_t rx = mk_rsrc(ws + O_X1, SZ_ACT), rw = mk_rsrc(ws + O_WQC, SZ_TOK), rv = mk_rsrc(ws + O_VOC, SZ_TOK);
    u32x4 xf[9], w4[9], vf[9][2]; u32x2 vh[9], xr[9];
    int dt0 = hf * 36 + wid * 9;
    poll(ws, lane < 18 ? FL_W + 2 * lane + b5 : FL_X + (rt5 >> 3) * 16 + (lane < 30 ? lane - 18 : 0));
    STAMP(1, 9);
#pragma unroll
    for (int s = 0; s < 9; s++) xf[s] = ld128<SC1>(rx, lane * 16 + s * BLK, (rt5 * KD + wid * 9) * BLK);
#pragma unroll
    for (int s = 0; s < 9; s++) w4[s] = ld128<SC1>(rw, lane * 16 + s * BLK, ((b5 * 5 + 4) * KD + wid * 9) * BLK);
#pragma unroll
    for (int n = 0; n < 9; n++) xr[n] = ld64<SC1>(rx, lane * 16 + ((dt0 + n) & 1) * 8, (rt5 * KD + ((dt0 + n) >> 1)) * BLK);
    SB();
    f32x2 xs[9][4], s1 = {0, 0};
#pragma unroll
    for (int s = 0; s < 9; s++)
#pragma unroll
      for (int j = 0; j < 4; j++) { xs[s][j] = f32x2{blo(xf[s][j]), bhi(xf[s][j])}; s1 += xs[s][j]; }
    float mw = qsum(s1[0] + s1[1]) * (1.f / 288);
    f32x2 s2 = {0, 0};
#pragma unroll
    for (int s = 0; s < 9; s++)
#pragma unroll
      for (int j = 0; j < 4; j++) { xs[s][j] -= mw; s2 += xs[s][j] * xs[s][j]; }
    float m2 = qsum(s2[0] + s2[1]);
    LDS f32x2* st = (LDS f32x2*)(sm + L_ST);
    if (q == 0) st[wid * 16 + r] = f32x2{mw, m2};
    SB();
    poll(ws, FL_W + 36 + (dt0 >> 2) + (lane & 3) - ((dt0 >> 2) + (lane & 3) > 17));
#pragma unroll
    for (int n = 0; n < 9; n++) {
      unsigned o = (b5 * 72 + dt0 + n) * 2560;
      vf[n][0] = ld128<SC1>(rv, lane * 16, o); vf[n][1] = ld128<SC1>(rv, lane * 16 + 1024, o); vh[n] = ld64<SC1>(rv, lane * 8 + 2048, o);
    }
    SB();
    wait_lgkm<0>(); bar();
    STAMP(1, 10);
    f32x2 e0 = st[r], e1 = st[16 + r], e2 = st[32 + r], e3 = st[48 + r];
    float mean = 0.25f * (e0[0] + e1[0] + e2[0] + e3[0]);
    float d0 = e0[0] - mean, d1 = e1[0] - mean, d2 = e2[0] - mean, d3 = e3[0] - mean;
    float rstd = rsqrtf((e0[1] + e1[1] + e2[1] + e3[1] + 288.f * (d0 * d0 + d1 * d1 + d2 * d2 + d3 * d3)) * (1.f / DM) + 1e-5f);
    float dm = mean - mw;
    f32x4 sc[5] = {};
    LDS char* wl = sm + wid * 36864 + lane * 16;
#pragma unroll
    for (int s = 0; s < 9; s++) {
      u32x4 hc;
#pragma unroll
      for (int j = 0; j < 4; j++) { f32x2 v = (xs[s][j] - dm) * rstd; hc[j] = pk2(v[0], v[1]); }
#pragma unroll
      for (int t = 0; t < 4; t++) sc[t] = mfma_b(*(LDS u32x4*)(wl + s * 4096 + t * 1024), hc, sc[t]);
      sc[4] = mfma_b(w4[s], hc, sc[4]);
    }
#pragma unroll
    for (int t = 0; t < 5; t++) *(LDS f32x4*)(wl + t * 1024) = sc[t];
    wait_lgkm<0>(); bar();
    STAMP(1, 11);
    LDS char* rl = sm + lane * 16;
#pragma unroll
    for (int t = 0; t < 5; t++)
      sc[t] = (*(LDS f32x4*)(rl + t * 1024) + *(LDS f32x4*)(rl + 36864 + t * 1024)) + (*(LDS f32x4*)(rl + 2 * 36864 + t * 1024) + *(LDS f32x4*)(rl + 3 * 36864 + t * 1024));
#pragma unroll
    for (int j = 1; j < 4; j++) sc[4][j] = q == 3 ? -__builtin_inff() : sc[4][j];
    float m = sc[0][0];
#pragma unroll
    for (int t = 0; t < 5; t++)
#pragma unroll
      for (int j = 0; j < 4; j++) m = fmaxf(m, sc[t][j]);
    m = qmax(m);
    float l = 0;
#pragma unroll
    for (int t = 0; t < 5; t++)
#pragma unroll
      for (int j = 0; j < 4; j++) { float p = __builtin_amdgcn_exp2f(sc[t][j] - m); sc[t][j] = p; l += p; }
    float inv = __builtin_amdgcn_rcpf(qsum(l));
    u32x4 p0 = pack8(sc[0] * inv, sc[1] * inv), p1 = pack8(sc[2] * inv, sc[3] * inv);
    u32x2 p2h = pack4(sc[4] * inv);
    u32x4 p2 = {p2h[0], p2h[1], 0u, 0u};
    rsrc_t ro = mk_rsrc(out, 2048 * 2304);
#pragma unroll
    for (int n = 0; n < 9; n++) {
      f32x4 o = {blo(xr[n][0]), bhi(xr[n][0]), blo(xr[n][1]), bhi(xr[n][1])};
      o = mfma_b(vf[n][0], p0, o);
      o = mfma_b(vf[n][1], p1, o);
      o = mfma_b(u32x4{vh[n][0], vh[n][1], 0u, 0u}, p2, o);
      st64(pack4(o), ro, (rt5 * 16 + r) * 2304 + q * 8, (dt0 + n) * 32);
    }
  }
  ENDSTAMP(1, 12);
#endif
}

constexpr int KF_CODE = 40 * 1024;

constexpr int K0_LDS = 147456 + 9216 + 768;
extern "C" __global__ __launch_bounds__(384) void k0_pre(const char* x, const char* c, const char* y, const char* wqkv, const char* wo,
                                                        const char* wqc, const char* wkvc, const char* woc, const char* wmod, char* ws) {
#if defined(__HIP_DEVICE_COMPILE__)
  extern __shared__ __attribute__((aligned(16))) char shm[];
  STAMP(0, 0);
  int bid = blockIdx.x, xcd = bid & 7, idx = bid >> 3;
  int wid = __builtin_amdgcn_readfirstlane(threadIdx.x >> 6), lane = threadIdx.x & 63;
  int qd = xcd & 3, nr = (qd & 1) ? 4 : 5, nc = (qd & 2) ? 4 : 5, r0 = (qd & 1) ? 5 : 0, c0 = (qd & 2) ? 5 : 0;
  LDS char* sm = (LDS char*)shm;
  unsigned l0 = (unsigned)(size_t)sm;
  int r = lane & 15, q = lane >> 4;
  if (idx < nr * nc) {
    int ti = r0 + idx % nr, tj = c0 + idx / nr, mat = xcd >> 2;
    int rot = (18 / K0PH) * ((ti + tj) % K0PH);
    constexpr int SLOT = 32768;
    if (wid >= 4) {
      RSrc<16> s;
      if (wid == 4) { s.r = mk_rsrc(wqkv, DM * 6912); s.kc(ti * 128 * 6912 + mat * 4608, 6912, lane); }
      else if (mat == 0) { s.r = mk_rsrc(wqkv, DM * 6912); s.kc(tj * 128 * 6912 + 2304, 6912, lane); }
      else { s.r = mk_rsrc(wo, DM * 2304); s.tr(tj * 256, 2304, lane); }
      LDS char* d = sm + (wid - 4) * 16384;
      r_loader<16>([&](int g, int slot) { s.issue(g, d + slot * SLOT); }, rot);
    } else {
      int wi = wid >> 1, wj = wid & 1;
      f32x4 acc[4][4]; zero(acc);
      Frag<KC, 4> fx; fx.init(l0, 0, wi * 4, lane);
      float sc = 1.f;
      if (mat == 0) { Frag<KC, 4> fy; fy.init(l0, 16384, wj * 4, lane); r_consumer(fx, fy, SLOT, acc); sc = QK_SCALE; }
      else { Frag<TR, 4> fy; fy.init(l0, 16384, wj * 4, lane); r_consumer(fx, fy, SLOT, acc); }
      STAMP(0, 2);
      rsrc_t ro = mk_rsrc(ws + O_W, 144 * KD * BLK);
      unsigned o = ((mat * 72 + tj * 8 + wj * 4) * KD + ti * 4 + wi * 2) * BLK;
#pragma unroll
      for (int j = 0; j < 4; j++) {
        st128<K0AUX>(pack8(acc[0][j] * sc, acc[1][j] * sc), ro, lane * 16, o + j * KD * BLK);
        st128<K0AUX>(pack8(acc[2][j] * sc, acc[3][j] * sc), ro, lane * 16 + 1024, o + j * KD * BLK);
      }
      ENDSTAMP(0, 3);
    }
    return;
  }
  int o = (xcd >> 2) * 47 + (qd == 0 ? 0 : qd == 1 ? 7 : qd == 2 ? 19 : 31) + idx - nr * nc;
  if (o < 72 && !(o & 1)) {
    constexpr int SLOT = 28 * 1024, WOFF = 20 * 1024;
    int pn = o >> 1, rot = (18 / K0PH) * (pn % K0PH);
    if (wid == 4) {
      RSrc<20> s; s.r = mk_rsrc(y, 154 * 2304); s.kstep = 128;
#pragma unroll
      for (int p = 0; p < 20; p++) {
        int row = 8 * p + (lane >> 3), b = row >= 80, t = row - 80 * b;
        s.v[p] = t < 77 ? (b * 77 + t) * 2304 + (((lane & 7) ^ ((row >> 1) & 7)) << 4) - (p & 3) * 1024 : OOB;
      }
      r_loader<20>([&](int g, int slot) { s.issue(g, sm + slot * SLOT); }, rot);
    } else if (wid == 5) {
      RSrc<8> s; s.r = mk_rsrc(wkvc, DM * 4608); s.tr(pn * 128, 4608, lane);
#pragma unroll 1
      for (int g = 0; g < 18; g++) s.issue(g, sm + 3 * SLOT + WOFF);
      r_loader<8>([&](int g, int slot) { s.issue(g, sm + slot * SLOT + WOFF); }, rot);
    } else {
      int b = wid >> 1, wn = wid & 1;
      Frag<KC, 5> ft; ft.init(l0, 0, b * 5, lane);
      Frag<TR, 2> fw; fw.init(l0, WOFF, wn * 2, lane);
      f32x4 acc[2][5]; zero(acc);
      r_consumer(fw, ft, SLOT, acc);
      STAMP(0, 2);
      rsrc_t ro = mk_rsrc(ws + O_KC, 2 * SZ_TOK);
      int kb = pn * 2 + wn, sel = kb >= KD;
      unsigned oo = sel * SZ_TOK + (kb - sel * KD) * BLK, vo = ((q >> 1) * 16 + r) * 16 + (q & 1) * 8;
#pragma unroll
      for (int t = 0; t < 5; t++) {
        st64(pack4(acc[0][t]), ro, vo, oo + (b * 5 + t) * KD * BLK);
        st64(pack4(acc[1][t]), ro, vo + 512, oo + (b * 5 + t) * KD * BLK);
      }
      ENDSTAMP(0, 3);
    }
    return;
  }
  int ax = o < 72 ? o >> 1 : o - 36;
  int pan = ax < 36 ? ax : ax + 14;
  GAS u64* flg = (GAS u64*)(ws + O_FLAG);
  u32x3 cv;
  { u32x4 dc = mk_desc(c, 2 * 2304); unsigned vo = (lane >> 5) * 2304 + wid * 384 + (lane & 31) * 12;
    asm volatile("buffer_load_dwordx3 %0, %1, %2, 0 offen" : "=v"(cv) : "v"(vo), "s"(dc) : "memory"); }
  auto wm_issue = [&]() {
    RSrc<8> s; s.tr(pan * 128 + wid * 192 * 13824, 13824, lane);
    u32x4 dw = mk_desc(wmod, DM * 13824);
#pragma unroll
    for (int g = 0; g < 3; g++) {
      dma4_asm(dw, l0 + wid * 24576 + g * 8192, s.v[0], s.v[1], s.v[2], s.v[3], g * 64 * 13824);
      dma4_asm(dw, l0 + wid * 24576 + g * 8192 + 4096, s.v[4], s.v[5], s.v[6], s.v[7], g * 64 * 13824);
    }
  };
  LDS float* red = (LDS float*)(sm + 147456);
  LDS unsigned* sil = (LDS unsigned*)(sm + 147456 + 3072);
  LDS float* tabf = (LDS float*)(sm + 147456);
  LDS f32x2* st = (LDS f32x2*)(sm + 147456 + 9216);
  auto wm_silu = [&](u32x4 (&cf)[6]) {
#pragma unroll
    for (int j = 0; j < 3; j++) {
      float a = blo(cv[j]), b = bhi(cv[j]);
      sil[wid * 192 + lane * 3 + j] = pk2(a * __builtin_amdgcn_rcpf(1.f + __expf(-a)), b * __builtin_amdgcn_rcpf(1.f + __expf(-b)));
    }
    wait_lgkm<0>();
    unsigned sa = l0 + 147456 + 3072 + wid * 768 + (r & 1) * 384 + q * 16;
    sfor<0, 6>([&](auto i) { dsr128<decltype(i)::value * 64>(cf[decltype(i)::value], sa); });
  };
  auto wm_mma = [&](u32x4 (&cf)[6]) {
    Frag<TR, 4> fw; fw.init(l0, wid * 24576, 0, lane);
    f32x4 acc[1][4]; zero(acc); pin2(acc);
    sfor<0, 3>([&](auto i) {
      constexpr int g = decltype(i)::value;
      u32x4 w[2][4];
      sfor<0, 4>([&](auto jj) { fw.template read<0, decltype(jj)::value>(w[0][decltype(jj)::value], g * 8192); fw.template read<1, decltype(jj)::value>(w[1][decltype(jj)::value], g * 8192); });
      wait_lgkm<0>();
#pragma unroll
      for (int h = 0; h < 2; h++) {
        pin(cf[2 * g + h]);
#pragma unroll
        for (int j = 0; j < 4; j++) { pin(w[h][j]); mfma(acc[0][j], w[h][j], cf[2 * g + h]); }
      }
    });
    mfma_done(); pin2(acc);
    if (r < 2) {
#pragma unroll
      for (int j = 0; j < 4; j++) *(LDS f32x4*)(red + wid * 128 + r * 64 + j * 16 + q * 4) = acc[0][j];
    }
    wait_lgkm<0>(); bar();
    if (wid == 0) {
      f32x4 a = {}, b = {};
#pragma unroll
      for (int w = 0; w < 6; w++) { a += *(LDS f32x4*)(red + w * 128 + r * 8); b += *(LDS f32x4*)(red + w * 128 + r * 8 + 4); }
      st128<SC1>(pack8(a, b), mk_rsrc(ws + O_MOD, 13824), lane < 16 ? (lane >> 3) * 6912 + pan * 128 + (lane & 7) * 16 : OOB, 0);
    }
  };
  u64 ep;
  asm volatile("global_load_dwordx2 %0, %1, %2 sc1" : "=v"(ep) : "v"(0u), "s"(flg) : "memory");
  if (ax < 40) wm_issue();
  SB();
  int hb = ax - 14, hbat = hb >= 22, hj = (hb - 22 * hbat) * 3 + (wid >> 1), ht = hbat * 64 + hj, kh = wid & 1;
  bool hval = hb >= 0 && hj < 64;
  u32x4 xv[18];
  { rsrc_t rx = mk_rsrc(x, 2048 * 2304);
    unsigned vo = hval ? (ht * 16 + r) * 2304 + kh * 1152 + q * 16 : OOB;
#pragma unroll
    for (int s = 0; s < 18; s++) xv[s] = ld128(rx, vo, s * 64); }
  SB();
  u32x4 cf[6];
  if (ax < 40) {
    asm volatile("s_waitcnt vmcnt(42)" : "+v"(cv), "+v"(ep)::"memory");
    wm_silu(cf);
    wait_vm<18>();
    wm_mma(cf);
    if (wid == 0) wait_vm<0>();
  } else asm volatile("s_waitcnt vmcnt(18)" : "+v"(cv), "+v"(ep)::"memory");
  u64 tag = mix64(ep);
  if (wid == 0 && lane == 0) __hip_atomic_store(flg + 8 + ax, tag, __ATOMIC_RELAXED, __HIP_MEMORY_SCOPE_AGENT);
  STAMP(0, 1);
  if (hb >= 0) {
    f32x2 xf[18][4], s1 = {0, 0};
#pragma unroll
    for (int s = 0; s < 18; s++)
#pragma unroll
      for (int j = 0; j < 4; j++) { xf[s][j] = f32x2{blo(xv[s][j]), bhi(xv[s][j])}; s1 += xf[s][j]; }
    float mw = qsum(s1[0] + s1[1]) * (1.f / 576);
    f32x2 s2 = {0, 0};
#pragma unroll
    for (int s = 0; s < 18; s++)
#pragma unroll
      for (int j = 0; j < 4; j++) { xf[s][j] -= mw; s2 += xf[s][j] * xf[s][j]; }
    float m2 = qsum(s2[0] + s2[1]);
    if (q == 0) st[wid * 16 + r] = f32x2{mw, m2};
    if (wid == 0) {
      int n = ax == 57 ? 58 : 36;
    #pragma unroll 1
  for (int spin = 0; spin < SPIN_CAP; spin++) {
        u64 f = __hip_atomic_load(flg + 8 + (lane < n ? lane : 0), __ATOMIC_RELAXED, __HIP_MEMORY_SCOPE_AGENT);
        if (__all(f == tag)) break;
        __builtin_amdgcn_s_sleep(1);
      }
      if (ax == 57 && lane == 0) __hip_atomic_store(flg, tag, __ATOMIC_RELAXED, __HIP_MEMORY_SCOPE_AGENT);
    }
    STAMP(0, 2);
    wait_lgkm<0>(); bar();
    int t = threadIdx.x;
    u32x4 mv = ld128<SC1>(mk_rsrc(ws + O_MOD, 13824), t < 288 ? t * 16 : OOB, hbat * 6912);
    f32x2 so = st[(wid ^ 1) * 16 + r];
    float dm = 0.5f * (so[0] - mw), rstd = rsqrtf((m2 + so[1] + 2 * 576 * dm * dm) * (1.f / DM) + 1e-5f);
#pragma unroll
    for (int s = 0; s < 18; s++)
#pragma unroll
      for (int j = 0; j < 4; j++) xf[s][j] = (xf[s][j] - dm) * rstd;
    if (t < 288) {
      float one = t >= 144 ? 1.f : 0.f;
      *(LDS f32x4*)(tabf + t * 8) = f32x4{blo(mv[0]), bhi(mv[0]), blo(mv[1]), bhi(mv[1])} + one;
      *(LDS f32x4*)(tabf + t * 8 + 4) = f32x4{blo(mv[2]), bhi(mv[2]), blo(mv[3]), bhi(mv[3])} + one;
    }
    wait_lgkm<0>(); bar();
    rsrc_t rh = mk_rsrc(ws + O_H, SZ_ACT);
    unsigned ho = hval ? (ht * KD + kh * 18) * BLK + ((((q & 1) << 1) | (q >> 1)) * 16 + r) * 16 : OOB;
    LDS float* tp = tabf + kh * 576 + q * 8;
#pragma unroll
    for (int s = 0; s < 18; s++) {
      f32x4 f0 = *(LDS f32x4*)(tp + s * 32), f1 = *(LDS f32x4*)(tp + s * 32 + 4), a0 = *(LDS f32x4*)(tp + 1152 + s * 32), a1 = *(LDS f32x4*)(tp + 1152 + s * 32 + 4);
      f32x2 h0 = xf[s][0] * a0.xy + f0.xy, h1 = xf[s][1] * a0.zw + f0.zw, h2 = xf[s][2] * a1.xy + f1.xy, h3 = xf[s][3] * a1.zw + f1.zw;
      auto e = __builtin_amdgcn_permlane32_swap(pk2(h0[0], h0[1]), pk2(h2[0], h2[1]), false, false);
      auto f = __builtin_amdgcn_permlane32_swap(pk2(h1[0], h1[1]), pk2(h3[0], h3[1]), false, false);
      st128<K0AUX>(u32x4{e[0], f[0], e[1], f[1]}, rh, ho, s * BLK);
    }
    STAMP(0, 4);
  } else {
    pan = 36 + ax;
    wait_lgkm<0>(); bar();
    wm_issue();
    wait_vm<0>();
    wm_mma(cf);
  }
  { int t = ax * 384 + threadIdx.x;
    __builtin_amdgcn_raw_buffer_store_b32(0u, mk_rsrc(ws + O_FL, FL_N * 128), t < FL_N ? t * 128 : OOB, 0, 0); }
  wait_lgkm<0>(); bar();
  {
    u64 pc;
    asm volatile("s_getpc_b64 s[40:41]\n\ts_add_u32 s40, s40, kf_main@rel32@lo+4\n\ts_addc_u32 s41, s41, kf_main@rel32@hi+12\n\ts_mov_b64 %0, s[40:41]" : "=s"(pc)::"s40", "s41", "scc");
    unsigned v = lane * 16;
    u32x4 dcode = mk_desc((const void*)pc, KF_CODE);
    if (idx - nr * nc < 2) for (int p = wid * 4096; p < KF_CODE; p += 6 * 4096) dma4_asm(dcode, l0 + wid * 4096, v, v, v, v, p);
    int first = (ax * 6 + wid) * 16;
    u32x4 d1 = mk_desc(wqc, DM * 2304), d2 = mk_desc(woc, DM * 2304);
    for (int p = first; p < first + 16; p += 4) {
      if (p < 2592) dma4_asm(d1, l0 + wid * 4096, v, v, v, v, p * 1024); else dma4_asm(d2, l0 + wid * 4096, v, v, v, v, (p - 2592) * 1024);
    }
  }
  ENDSTAMP(0, 3);
#endif
}

extern "C" void kernel_launch(void* const* d_in, const int* in_sizes, int n_in, void* d_out, int out_size, void* d_ws, size_t ws_size,
                              hipStream_t stream) {
  const char *x = (const char*)d_in[0], *c = (const char*)d_in[1], *y = (const char*)d_in[2], *wqkv = (const char*)d_in[3], *wo = (const char*)d_in[4],
             *wqc = (const char*)d_in[5], *wkvc = (const char*)d_in[6], *woc = (const char*)d_in[7], *wmod = (const char*)d_in[8];
  char* ws = (char*)d_ws;
  (void)hipFuncSetAttribute((const void*)k0_pre, hipFuncAttributeMaxDynamicSharedMemorySize, K0_LDS);
  (void)hipFuncSetAttribute((const void*)kf_main, hipFuncAttributeMaxDynamicSharedMemorySize, KF_LDS);
  k0_pre<<<256, 384, K0_LDS, stream>>>(x, c, y, wqkv, wo, wqc, wkvc, woc, wmod, ws);
  kf_main<<<256, 320, KF_LDS, stream>>>(x, wqc, woc, ws, (char*)d_out);
}
```

```cpp
#define PROF 1
#include <hip/hip_runtime.h>
#include <hip/hip_bf16.h>
#include <type_traits>

typedef __attribute__((ext_vector_type(8))) __bf16 bf16x8;
typedef __attribute__((ext_vector_type(2))) __bf16 bf16x2;
typedef __attribute__((ext_vector_type(4))) float f32x4;
typedef __attribute__((ext_vector_type(2))) float f32x2;
typedef __attribute__((ext_vector_type(4))) unsigned u32x4;
typedef __attribute__((ext_vector_type(3))) unsigned u32x3;
typedef __attribute__((ext_vector_type(2))) unsigned u32x2;
typedef unsigned long long u64;
#define LDS __attribute__((address_space(3)))
#define GAS __attribute__((address_space(1)))
#define DI __device__ __forceinline__

constexpr int DM = 1152;
constexpr int KD = 36;
constexpr int BLK = 1024;
constexpr size_t O_FLAG = 0;
constexpr size_t O_MOD = 4096;
constexpr size_t O_FL = 32768;
constexpr int FL_T = 0, FL_V = 512, FL_W = 1024, FL_O = 1096, FL_N = 1168;
constexpr size_t O_ST = O_FL + 1280 * 128;
constexpr size_t O_XS = O_ST + 128 * 16 * 128;
constexpr size_t CLR_BYTES = 1280 * 128 + 128 * 16 * 128 + 128 * 12 * 128;
constexpr size_t O_H = O_FL + CLR_BYTES;
constexpr size_t SZ_ACT = 128ull * 36 * 1024;
constexpr size_t O_W = O_H + SZ_ACT;
constexpr size_t O_T = O_W + 144ull * 36 * 1024;
constexpr size_t O_V = O_T + SZ_ACT;
constexpr size_t O_P = O_V + SZ_ACT;
constexpr size_t O_X1 = O_P + 128ull * 32 * 1024;
constexpr size_t SZ_TOK = 10ull * 36 * 1024;
constexpr size_t O_KC = O_X1 + SZ_ACT;
constexpr size_t O_VC = O_KC + SZ_TOK;
constexpr size_t O_WQC = O_VC + SZ_TOK;
constexpr size_t O_VOC = O_WQC + SZ_TOK;

constexpr size_t O_PROF = 200ull << 20;
#define STAMP(k, i) do { if (__builtin_amdgcn_readfirstlane(threadIdx.x) < 64) { ((GAS u64*)(ws + O_PROF))[((k) * 256 + blockIdx.x) * 32 + (i)] = __builtin_amdgcn_s_memrealtime(); } } while (0)
#define STAMPL(i) do { ((GAS u64*)(ws + O_PROF))[(256 + blockIdx.x) * 32 + (i)] = __builtin_amdgcn_s_memrealtime(); } while (0)
#define ENDSTAMP(k, i) do { wait_vm<0>(); STAMP(k, i); } while (0)
#define VTHR 63
#define PH2 0
#define VDELAY 24
#define K0AUX 16
#define K0PH 4

constexpr float QK_SCALE = 1.4426950408889634f * 0.029462782549439476f;

#if defined(__HIP_DEVICE_COMPILE__)
typedef __amdgpu_buffer_rsrc_t rsrc_t;
DI rsrc_t mk_rsrc(const void* p, unsigned bytes) { return __builtin_amdgcn_make_buffer_rsrc((void*)p, 0, bytes, 0x00020000); }
DI u32x4 mk_desc(const void* p, unsigned bytes) { u64 a = (u64)p; return u32x4{(unsigned)a, (unsigned)(a >> 32) & 0xffffu, bytes, 0x00020000u}; }
DI unsigned pk2(float a, float b) { f32x2 p = {a, b}; return __builtin_bit_cast(unsigned, __builtin_convertvector(p, bf16x2)); }
DI float blo(unsigned u) { return __uint_as_float(u << 16); }
DI float bhi(unsigned u) { return __uint_as_float(u & 0xffff0000u); }
template <int N> DI void wait_vm() { asm volatile("s_waitcnt vmcnt(%0)" ::"n"(N > 63 ? 63 : N) : "memory"); }
template <int N> DI void wait_lgkm() { asm volatile("s_waitcnt lgkmcnt(%0)" ::"n"(N > 15 ? 15 : N) : "memory"); }
DI void SB() { __builtin_amdgcn_sched_barrier(0); }
DI void bar() { asm volatile("s_barrier" ::: "memory"); }
template <int I, int N, class F> DI void sfor(F&& f) { if constexpr (I < N) { f(std::integral_constant<int, I>{}); sfor<I + 1, N>(f); } }
template <int V> using ic = std::integral_constant<int, V>;
DI void mfma(f32x4& c, const u32x4& x, const u32x4& y) { asm volatile("v_mfma_f32_16x16x32_bf16 %0, %1, %2, %0" : "+v"(c) : "v"(x), "v"(y)); }
DI f32x4 mfma_b(const u32x4& x, const u32x4& y, const f32x4& c) {
  return __builtin_amdgcn_mfma_f32_16x16x32_bf16(__builtin_bit_cast(bf16x8, x), __builtin_bit_cast(bf16x8, y), c, 0, 0, 0);
}
template <int OFF> DI void dsr128(u32x4& d, unsigned a) { asm volatile("ds_read_b128 %0, %1 offset:%2" : "=v"(d) : "v"(a), "n"(OFF)); }
template <int OFF> DI void dsr_tr(u32x2& d, unsigned a) { asm volatile("ds_read_b64_tr_b16 %0, %1 offset:%2" : "=v"(d) : "v"(a), "n"(OFF)); }
constexpr int SC1 = 16;
template <int AUX = 0> DI u32x4 ld128(rsrc_t r, unsigned v, unsigned s) { return __builtin_amdgcn_raw_buffer_load_b128(r, v, s, AUX); }
template <int AUX = 0> DI u32x2 ld64(rsrc_t r, unsigned v, unsigned s) { return __builtin_amdgcn_raw_buffer_load_b64(r, v, s, AUX); }
template <int AUX = 0> DI void st128(u32x4 d, rsrc_t r, unsigned v, unsigned s) {
  __builtin_amdgcn_raw_buffer_store_b128(d, r, v, s, AUX); SB(); asm volatile("s_nop 1"); SB();
}
template <int AUX = 0> DI void st64(u32x2 d, rsrc_t r, unsigned v, unsigned s) { __builtin_amdgcn_raw_buffer_store_b64(d, r, v, s, AUX); }
template <int IMM, int AUX = 0> DI void dma16(rsrc_t r, LDS char* d, unsigned v, unsigned s) { __builtin_amdgcn_raw_ptr_buffer_load_lds(r, d, 16, v, s, IMM, AUX); }
DI void dma4_asm(u32x4 desc, unsigned lds, unsigned v0, unsigned v1, unsigned v2, unsigned v3, unsigned s) {
  unsigned keep;
  asm volatile("s_mov_b32 %0, m0\n\ts_mov_b32 m0, %2\n\ts_nop 0\n\t"
               "buffer_load_dwordx4 %3, %1, %7 offen lds\n\tbuffer_load_dwordx4 %4, %1, %7 offen offset:1024 lds\n\t"
               "buffer_load_dwordx4 %5, %1, %7 offen offset:2048 lds\n\tbuffer_load_dwordx4 %6, %1, %7 offen offset:3072 lds\n\ts_mov_b32 m0, %0"
               : "=&s"(keep) : "s"(desc), "s"(lds), "v"(v0), "v"(v1), "v"(v2), "v"(v3), "s"(s) : "memory");
}
DI u32x4 pack8(const f32x4& a, const f32x4& b) { return u32x4{pk2(a[0], a[1]), pk2(a[2], a[3]), pk2(b[0], b[1]), pk2(b[2], b[3])}; }
DI u32x2 pack4(const f32x4& a) { return u32x2{pk2(a[0], a[1]), pk2(a[2], a[3])}; }
DI void mfma_done() { asm volatile("s_nop 15" ::: "memory"); }
template <class T> DI void pin(T& x) { asm volatile("" : "+v"(x)); }
DI float qmax(float x) {
  auto a = __builtin_amdgcn_permlane32_swap(__float_as_uint(x), __float_as_uint(x), false, false);
  x = fmaxf(__uint_as_float(a[0]), __uint_as_float(a[1]));
  auto b = __builtin_amdgcn_permlane16_swap(__float_as_uint(x), __float_as_uint(x), false, false);
  return fmaxf(__uint_as_float(b[0]), __uint_as_float(b[1]));
}
DI float qsum(float x) {
  auto a = __builtin_amdgcn_permlane32_swap(__float_as_uint(x), __float_as_uint(x), false, false);
  x = __uint_as_float(a[0]) + __uint_as_float(a[1]);
  auto b = __builtin_amdgcn_permlane16_swap(__float_as_uint(x), __float_as_uint(x), false, false);
  return __uint_as_float(b[0]) + __uint_as_float(b[1]);
}
constexpr unsigned OOB = 0x7ff00000u;
constexpr int SPIN_CAP = 1 << 18;
template <int NX, int NY> DI void zero(f32x4 (&a)[NX][NY]) {
#pragma unroll
  for (int i = 0; i < NX; i++)
#pragma unroll
    for (int j = 0; j < NY; j++) a[i][j] = f32x4{0, 0, 0, 0};
}
template <int NX, int NY> DI void pin2(f32x4 (&a)[NX][NY]) {
#pragma unroll
  for (int i = 0; i < NX; i++)
#pragma unroll
    for (int j = 0; j < NY; j++) pin(a[i][j]);
}

DI void publish(char* ws, int flag, int lane) {
  wait_vm<0>();
  __builtin_amdgcn_raw_buffer_store_b32(1u, mk_rsrc(ws + O_FL, FL_N * 128), lane == 0 ? 0u : OOB, flag * 128, SC1);
}
DI void poll(const char* ws, int flag_of_lane) {
  GAS const unsigned* p = (GAS const unsigned*)(ws + O_FL) + flag_of_lane * 32;
#pragma unroll 1
  for (int spin = 0; spin < SPIN_CAP; spin++) {
    unsigned v = __hip_atomic_load(p, __ATOMIC_RELAXED, __HIP_MEMORY_SCOPE_AGENT);
    if (__all(v == 1u)) break;
    __builtin_amdgcn_s_sleep(1);
  }
  asm volatile("" ::: "memory"); SB();
}

DI bool ready(const char* ws, int flag_of_lane) {
  unsigned v = __hip_atomic_load((GAS const unsigned*)(ws + O_FL) + flag_of_lane * 32, __ATOMIC_RELAXED, __HIP_MEMORY_SCOPE_AGENT);
  bool ok = __all(v == 1u);
  asm volatile("" ::: "memory"); SB();
  return ok;
}

template <int NT, int KS, int D, bool PERG, int START, int WRAP, bool SWAP, bool SCALE, int AUX, class PRE>
DI void f_consumer(rsrc_t ra, unsigned a0, unsigned a1, unsigned lp, int pb, f32x4 (&acc)[2][NT], unsigned tab, PRE&& pre, char* ws = nullptr, int sb = -1) {
  constexpr int U = D / 2, NB = KS / D, SLOT = NT * 2048, REG = U * SLOT;
  static_assert(KS % D == 0, "");
  u32x4 a[D][2];
  unsigned sa = pb * D * BLK;
#pragma unroll
  for (int d = 0; d < D - 1; d++) { a[d][0] = ld128<AUX>(ra, a0, sa + d * BLK); SB(); a[d][1] = ld128<AUX>(ra, a1, sa + d * BLK); SB(); }
  u32x4 b0[NT], b1[NT], sh[2], shn[2];
  auto scale = [&](u32x4& v, int j, unsigned s) { asm volatile("v_pk_sub_u16 %0, %0, %1 clamp" : "+v"(v[j]) : "v"(s)); };
  pre();
  if constexpr (SCALE) {
    dsr128<0>(sh[0], tab); dsr128<1024>(sh[1], tab);
    wait_lgkm<0>(); pin(sh[0]); pin(sh[1]);
#pragma unroll
    for (int j = 0; j < 4; j++) { scale(a[0][0], j, sh[0][0]); scale(a[0][1], j, sh[1][0]); }
  }
  auto half = [&](u32x4 (&av)[2], u32x4 (&bc)[NT], u32x4 (&bn)[NT], unsigned addr, auto off, u32x4 (&ar)[2], unsigned so, auto nx,
                  u32x4 (&an)[2], unsigned s0, unsigned s1) {
    sfor<0, NT>([&](auto i) {
      constexpr int I = decltype(i)::value, WS = NT == 4 ? 2 : 3;
      if constexpr (I % WS == 0) { constexpr int need = (I + WS < NT ? I + WS : NT); wait_lgkm<NT - need + I + decltype(nx)::value>(); }
      if constexpr (SWAP) mfma(acc[0][I], av[0], bc[I]); else mfma(acc[0][I], bc[I], av[0]);
      dsr128<I * 2048 + decltype(off)::value>(bn[I], addr);
      if constexpr (SWAP) mfma(acc[1][I], av[1], bc[I]); else mfma(acc[1][I], bc[I], av[1]);
      if constexpr (I == 0) { SB(); ar[0] = ld128<AUX>(ra, a0, so); SB(); }
      if constexpr (I == 2) { SB(); ar[1] = ld128<AUX>(ra, a1, so); SB(); }
      if constexpr (SCALE && I >= 1 && I <= 4) { scale(an[(I - 1) >> 1], ((I - 1) & 1) * 2, I < 3 ? s0 : s1); scale(an[(I - 1) >> 1], ((I - 1) & 1) * 2 + 1, I < 3 ? s0 : s1); }
    });
  };
  pin2(acc);
  SB();
  bar();
  if (sb >= 0) STAMP(1, sb);
  unsigned cur = lp + START; int reg = START;
  sfor<0, NT>([&](auto i) { dsr128<decltype(i)::value * 2048>(b0[decltype(i)::value], cur); });
#pragma unroll 1
  for (int it = 0; it < NB; it++) {
    bool last = it == NB - 1;
    pb = pb + 1 == NB ? 0 : pb + 1;
    unsigned sn = last ? OOB : pb * D * BLK;
    reg = reg + REG == WRAP ? 0 : reg + REG;
    unsigned nxt = lp + reg;
    if constexpr (SCALE) { tab += 16; dsr128<0>(shn[0], tab); dsr128<1024>(shn[1], tab); }
    sfor<0, U>([&](auto uu) {
      constexpr int u = decltype(uu)::value;
      half(a[2 * u], b0, b1, cur, ic<u * SLOT + 1024>{}, a[(2 * u + D - 1) % D], u == 0 ? sa + (D - 1) * BLK : sn + (2 * u - 1) * BLK,
           ic<(SCALE && u == 0) ? 2 : 0>{}, a[2 * u + 1], sh[0][u & 3], sh[1][u & 3]);
      if constexpr (u < U - 1) {
        if constexpr (PERG) bar();
        half(a[2 * u + 1], b1, b0, cur, ic<(u + 1) * SLOT>{}, a[2 * u], sn + 2 * u * BLK, ic<0>{}, a[2 * u + 2], sh[0][(u + 1) & 3], sh[1][(u + 1) & 3]);
      } else {
        if (!last) bar();
        if (sb >= 0 && it < 3) STAMP(1, sb + 1 + it);
        if constexpr (SCALE) { pin(shn[0]); pin(shn[1]); }
        half(a[2 * u + 1], b1, b0, nxt, ic<0>{}, a[2 * u], sn + 2 * u * BLK, ic<0>{}, a[0], shn[0][0], shn[1][0]);
      }
    });
    if constexpr (SCALE) { sh[0] = shn[0]; sh[1] = shn[1]; }
    cur = nxt; sa = sn;
  }
  wait_lgkm<0>();
  mfma_done();
  pin2(acc);
}
template <int NT, int KS, int AUX> struct FSrc {
  rsrc_t r; unsigned v[NT]; int rot;
  DI void init(rsrc_t rb, unsigned boff, int rot_, int lane) {
    r = rb; rot = rot_;
#pragma unroll
    for (int n = 0; n < NT; n++) v[n] = boff + lane * 16 + n * KS * BLK - (n & 1) * 2048;
  }
  DI void issue(int g, LDS char* dst) const {
    int gg = g + rot; gg = gg >= KS / 2 ? gg - KS / 2 : gg;
    unsigned so = gg * 2048;
    sfor<0, NT>([&](auto i) {
      constexpr int n = decltype(i)::value;
      dma16<(n & 1) * 2048, AUX>(r, dst + (n >> 1) * 4096, v[n], so);
      dma16<(n & 1) * 2048 + 1024, AUX>(r, dst + (n >> 1) * 4096, v[n], so);
    });
  }
};

enum { KC = 0, TR = 1, FL = 2 };
template <int NP> struct RSrc {
  rsrc_t r; unsigned v[NP]; unsigned kstep;
  DI void kc(unsigned base, unsigned stride, int lane) {
#pragma unroll
    for (int p = 0; p < NP; p++) { int row = 8 * p + (lane >> 3); v[p] = base + row * stride + (((lane & 7) ^ ((row >> 1) & 7)) << 4) - (p & 3) * 1024; }
    kstep = 128;
  }
  DI void tr(unsigned base, unsigned stride, int lane) {
#pragma unroll
    for (int p = 0; p < NP; p++) {
      int row = 8 * (p & 7) + (lane >> 3), f = ((row >> 1) & 1) | (((row >> 3) & 1) << 1);
      v[p] = base + (p >> 3) * 128 + row * stride + (((lane & 7) ^ (f << 1)) << 4) - (p & 3) * 1024;
    }
    kstep = 64 * stride;
  }
  DI void fl(unsigned base, int lane) {
#pragma unroll
    for (int p = 0; p < NP; p++) v[p] = base + (p >> 1) * KD * BLK + (p & 1) * BLK + lane * 16 - (p & 3) * 1024;
    kstep = 2048;
  }
  DI void issue(int gg, LDS char* dst) const {
    unsigned so = gg * kstep;
    sfor<0, NP>([&](auto i) { constexpr int p = decltype(i)::value; dma16<(p & 3) * 1024>(r, dst + (p >> 2) * 4096, v[p], so); });
  }
};
template <int NP, class F> DI void r_loader(F&& iss, int rot) {
  constexpr int G = 18;
  auto issue = [&](int g, int slot) { int gg = g + rot; iss(gg >= G ? gg - G : gg, slot); };
  issue(0, 0); issue(1, 1);
  wait_vm<NP>(); bar();
  issue(2, 2); issue(3, 3);
  int slot = 0;
#pragma unroll 1
  for (int g = 1; g < G - 3; g++) {
    wait_vm<2 * NP>(); bar();
    issue(g + 3, slot); slot = (slot + 1) & 3;
  }
  wait_vm<2 * NP>(); bar();
  wait_vm<NP>(); bar();
  wait_vm<0>(); bar();
}
template <int KIND, int NTL> struct Frag {
  unsigned base[KIND == TR ? NTL : (KIND == KC ? 2 : 1)];
  DI void init(unsigned lds0, int ioff, int t0, int lane) {
    int r = lane & 15, q = lane >> 4;
    if constexpr (KIND == KC) {
      unsigned b = lds0 + ioff + (t0 * 16 + r) * 128;
      base[0] = b + ((q ^ ((r >> 1) & 7)) << 4); base[1] = b + (((4 + q) ^ ((r >> 1) & 7)) << 4);
    } else if constexpr (KIND == TR) {
      int qq = r >> 2, p = r & 3, f = ((qq >> 1) & 1) | ((q & 1) << 1);
#pragma unroll
      for (int c = 0; c < NTL; c++) { int t = t0 + c; base[c] = lds0 + ioff + (t >> 2) * 8192 + (8 * q + qq) * 128 + (((t & 3) ^ f) << 5) + p * 8; }
    } else base[0] = lds0 + ioff + t0 * 2048 + lane * 16;
  }
  template <int H, int I> DI void read(u32x4& d, unsigned so) const {
    if constexpr (KIND == KC) dsr128<I * 2048>(d, base[H] + so);
    else if constexpr (KIND == FL) dsr128<I * 2048 + H * 1024>(d, base[0] + so);
    else {
      u32x2 lo, hi;
      dsr_tr<H * 4096>(lo, base[I] + so); dsr_tr<H * 4096 + 512>(hi, base[I] + so);
      d = u32x4{lo[0], lo[1], hi[0], hi[1]};
    }
  }
};
template <int KX, int NX, int KY, int NY>
DI void r_consumer(const Frag<KX, NX>& fx, const Frag<KY, NY>& fy, int slot_bytes, f32x4 (&acc)[NX][NY]) {
  u32x4 xa[2][NX], ya[2][NY], xb[2][NX], yb[2][NY];
  auto rd = [&](u32x4 (&xn)[2][NX], u32x4 (&yn)[2][NY], unsigned so, auto i) {
    constexpr int I = decltype(i)::value, H = I / (NX + NY), J = I % (NX + NY);
    if constexpr (J < NY) fy.template read<H, J>(yn[H][J], so); else fx.template read<H, J - NY>(xn[H][J - NY], so);
  };
  auto land = [&](u32x4 (&xn)[2][NX], u32x4 (&yn)[2][NY]) {
    wait_lgkm<0>();
#pragma unroll
    for (int h = 0; h < 2; h++) {
#pragma unroll
      for (int i = 0; i < NX; i++) pin(xn[h][i]);
#pragma unroll
      for (int i = 0; i < NY; i++) pin(yn[h][i]);
    }
  };
  auto step = [&](u32x4 (&xc)[2][NX], u32x4 (&yc)[2][NY], u32x4 (&xn)[2][NX], u32x4 (&yn)[2][NY], unsigned so, auto more) {
    sfor<0, 2 * NX * NY>([&](auto i) {
      constexpr int I = decltype(i)::value, H = I / (NX * NY), J = I % (NX * NY);
      mfma(acc[J / NY][J % NY], xc[H][J / NY], yc[H][J % NY]);
      if constexpr (decltype(more)::value && I < 2 * (NX + NY)) rd(xn, yn, so, i);
    });
    if constexpr (decltype(more)::value) land(xn, yn);
  };
  pin2(acc);
  SB();
  bar();
  sfor<0, 2 * (NX + NY)>([&](auto i) { rd(xa, ya, 0, i); });
  land(xa, ya);
  int slot = 0;
#pragma unroll 1
  for (int g = 0; g < 8; g++) {
    bar();
    step(xa, ya, xb, yb, (slot + 1) * slot_bytes, ic<1>{});
    slot ^= 2;
    bar();
    step(xb, yb, xa, ya, slot * slot_bytes, ic<1>{});
  }
  bar();
  step(xa, ya, xb, yb, slot_bytes, ic<1>{});
  step(xb, yb, xa, ya, 0, ic<0>{});
  mfma_done();
  pin2(acc);
}
DI u64 mix64(u64 z) { z += 0x9E3779B97F4A7C15ull; z = (z ^ (z >> 30)) * 0xBF58476D1CE4E5B9ull; z = (z ^ (z >> 27)) * 0x94D049BB133111EBull; return z ^ (z >> 31); }
#endif

constexpr int R0 = 98304, L_TAB = 147456, KF_LDS = L_TAB + 8192 + 64;
extern "C" __global__ __launch_bounds__(320) void kf_main(const char* x, const char* wqc, const char* woc, char* ws, char* out) {
#if defined(__HIP_DEVICE_COMPILE__)
  extern __shared__ __attribute__((aligned(16))) char shm[];
  STAMP(1, 0);
  const int bid = blockIdx.x, xcd = bid & 7, idx = bid >> 3;
  const int wid = __builtin_amdgcn_readfirstlane(threadIdx.x >> 6), lane = threadIdx.x & 63, r = lane & 15, q = lane >> 4;
  LDS char* sm = (LDS char*)shm;
  const unsigned l0 = (unsigned)(size_t)sm;
  const int b = xcd >> 2;
  const bool isT = idx < 16;
  const int cp = 2 * (xcd & 3) + ((idx >> 3) & 1), rp2 = b * 8 + (idx & 7), ph2 = PH2;
  const int rp = 2 * xcd + (idx & 1), kg = idx >> 1, ph3 = kg % 3;
  const int rp4 = b * 8 + (xcd & 2) * 2 + (idx & 3), dp = (xcd & 1) * 6 + (idx >> 2), ph4 = (idx >> 2) & 3;
  const int so = (idx - 24) * 8 + xcd;
  const int rt5 = xcd * 16 + (idx >> 1), hf = idx & 1;
  constexpr int TSLOT = 28 * 1024, TW = 20 * 1024;

  if (wid == 4) {
    FSrc<4, KD, 0> h; h.init(mk_rsrc(ws + O_H, SZ_ACT), (b * 64 + kg * 4) * KD * BLK, ph3 * 6, lane);
    {
      FSrc<9, KD, 0> w; w.init(mk_rsrc(ws + O_W, 144 * KD * BLK), ((isT ? 0 : 72) + cp * 9) * KD * BLK, ph2 * 3, lane);
      w.issue(0, sm); w.issue(1, sm + 18432);
      wait_vm<18>(); bar();
      w.issue(2, sm + 2 * 18432); w.issue(3, sm + 3 * 18432);
      int slot = 4;
#pragma unroll 1
      for (int g = 1; g < 15; g++) {
        wait_vm<36>(); bar();
        w.issue(g + 3, sm + slot * 18432); slot = slot == 5 ? 0 : slot + 1;
      }
      wait_vm<36>(); bar();
      h.issue(2, sm + R0 + 2 * 8192); h.issue(3, sm + R0 + 3 * 8192);
      wait_vm<34>(); bar();
      h.issue(4, sm + R0 + 4 * 8192); h.issue(5, sm + R0 + 5 * 8192);
      wait_vm<32>(); bar();
      bar();
      h.issue(0, sm + R0); h.issue(1, sm + R0 + 8192);
#pragma unroll 1
      for (int g = 6; g < 18; g++) { h.issue(g, sm + (g - 6) * 8192); wait_vm<16>(); }
      wait_vm<0>(); STAMPL(21); bar();
      bar();
      bar();
    }
    const int fw_lane = FL_W + (lane < 36 ? (lane >> 1) * 4 + (lane & 1) : 0) + b * 2;
    rsrc_t rw = mk_rsrc(ws + O_WQC, SZ_TOK);
    unsigned v0 = b * 5 * KD * BLK + lane * 16, v1 = v0 + KD * BLK - 1024, v2 = v0 + 2 * KD * BLK - 2048, v3 = v0 + 3 * KD * BLK - 3072;
    auto wq_issue = [&](int s) {
      dma16<0, SC1>(rw, sm + s * 4096, v0, s * BLK); dma16<1024, SC1>(rw, sm + s * 4096, v1, s * BLK);
      dma16<2048, SC1>(rw, sm + s * 4096, v2, s * BLK); dma16<3072, SC1>(rw, sm + s * 4096, v3, s * BLK);
    };
    int wq_done = 0;
    if (idx < 24) {
      const int fv_lane = FL_V + ((lane & 32 ? (dp * 6 + 5) / 9 : (dp * 6) / 9) * 16 + b * 8) * 4 + (lane & 31);
      FSrc<6, 32, SC1> v; v.init(mk_rsrc(ws + O_V, SZ_ACT), (b * 72 + dp * 6) * 32 * BLK, ph4 * 4, lane);
      int g = 0;
      if (ready(ws, fv_lane)) {
#pragma unroll 1
        for (; g < 7; g++) { v.issue(g, sm + (g < 4 ? R0 + g * 12288 : (g - 4) * 12288)); wait_vm<24>(); }
      }
      STAMPL(22);
      bar();
      if (g == 0) poll(ws, fv_lane);
#pragma unroll 1
      for (; g < 12; g++) { v.issue(g, sm + (g < 4 ? R0 + g * 12288 : (g - 4) * 12288)); wait_vm<VTHR>(); }
      wait_vm<12>();
      STAMPL(23);
      bar();
      bar();
      wait_vm<0>(); bar();
#pragma unroll 1
      for (int g = 12; g < 16; g++) v.issue(g, sm + R0 + (g - 12) * 12288);
      bool rdy = ready(ws, fw_lane);
      wait_vm<0>(); bar();
      if (rdy) {
#pragma unroll 1
        for (int s = 0; s < 21; s++) wq_issue(s);
        wq_done = 21;
      }
      bar();
    } else {
      bar();
      if (so < 36) {
        RSrc<10> t; t.r = mk_rsrc(ws + O_KC, SZ_TOK); t.fl((so & 1) * 5 * KD * BLK, lane);
        RSrc<8> w; w.r = mk_rsrc(wqc, DM * 2304); w.kc((so >> 1) * 64 * 2304, 2304, lane);
        r_loader<18>([&](int g, int slot) { t.issue(g, sm + slot * TSLOT); w.issue(g, sm + slot * TSLOT + TW); }, 0);
        bar();
      } else if (so < 54) {
        RSrc<20> t; t.r = mk_rsrc(ws + O_VC, SZ_TOK); t.fl(0, lane);
        RSrc<8> w; w.r = mk_rsrc(woc, DM * 2304); w.tr((so - 36) * 128, 2304, lane);
        r_loader<28>([&](int g, int slot) { t.issue(g, sm + slot * TSLOT); w.issue(g, sm + slot * TSLOT + TW); }, 0);
        bar();
      }
    }
    if (wq_done == 0) poll(ws, fw_lane);
#pragma unroll 1
    for (int s = wq_done; s < KD; s++) wq_issue(s);
    wait_vm<0>(); STAMPL(24); bar();
    return;
  }

  const unsigned lp = l0 + lane * 16;
  {
    rsrc_t ra = mk_rsrc(ws + O_H, SZ_ACT);
    unsigned a0 = (rp2 * 8 + wid * 2) * KD * BLK + lane * 16, a1 = a0 + KD * BLK;
    f32x4 acc[2][9]; zero(acc);
    if (isT) {
      asm volatile("; T" : "+v"(a0), "+v"(a1));
      f_consumer<9, KD, 6, true, 0, 110592, false, false, 0>(ra, a0, a1, lp, ph2, acc, 0, [] {});
      STAMP(1, 1);
      rsrc_t rt = mk_rsrc(ws + O_T, SZ_ACT);
      unsigned ob = (rp2 * 8 + wid * 2) * KD * BLK + lane * 16 + ((cp * 9) >> 1) * BLK;
      if (cp & 1) {
#pragma unroll
        for (int t = 0; t < 2; t++) {
          st64<SC1>(pack4(acc[t][0]), rt, ob + 8, t * KD * BLK);
#pragma unroll
          for (int p = 0; p < 4; p++) st128<SC1>(pack8(acc[t][1 + 2 * p], acc[t][2 + 2 * p]), rt, ob + (p + 1) * BLK, t * KD * BLK);
        }
      } else {
#pragma unroll
        for (int t = 0; t < 2; t++) {
#pragma unroll
          for (int p = 0; p < 4; p++) st128<SC1>(pack8(acc[t][2 * p], acc[t][2 * p + 1]), rt, ob + p * BLK, t * KD * BLK);
          st64<SC1>(pack4(acc[t][8]), rt, ob + 4 * BLK, t * KD * BLK);
        }
      }
      SB(); bar();
    } else {
      asm volatile("; V" : "+v"(a0), "+v"(a1));
      f_consumer<9, KD, 6, true, 0, 110592, true, false, 0>(ra, a0, a1, lp, ph2, acc, 0, [] {});
      STAMP(1, 1);
      bar();
      __builtin_amdgcn_s_sleep(VDELAY);
      rsrc_t rv = mk_rsrc(ws + O_V, SZ_ACT);
      unsigned ob = ((b * 72 + cp * 9) * 32 + (rp2 & 7) * 4 + wid) * BLK + lane * 16;
#pragma unroll
      for (int n = 0; n < 9; n++) st128<SC1>(pack8(acc[0][n], acc[1][n]), rv, ob, n * 32 * BLK);
    }
    publish(ws, (isT ? FL_T + (rp2 * 8 + cp) * 4 : FL_V + (cp * 16 + rp2) * 4) + wid, lane);
    STAMP(1, 2);
  }
  {
    const int rt0 = rp * 8 + wid * 2;
    f32x4 acc[2][4]; zero(acc);
    poll(ws, FL_T + (rp * 8 + (lane & 7)) * 4 + wid);
    STAMP(1, 3);
    unsigned a0 = rt0 * KD * BLK + lane * 16, a1 = a0 + KD * BLK;
    f_consumer<4, KD, 12, false, R0, 147456, false, false, SC1>(mk_rsrc(ws + O_T, SZ_ACT), a0, a1, lp, ph3, acc, 0, [] {}, ws, 17);
    STAMP(1, 4);
    rsrc_t rpp = mk_rsrc(ws + O_P, 128 * 32 * BLK), rs = mk_rsrc(ws + O_ST, 128 * 16 * 128);
    u32x2 rec[2];
#pragma unroll
    for (int t = 0; t < 2; t++) {
      float m = acc[t][0][0];
#pragma unroll
      for (int n = 0; n < 4; n++)
#pragma unroll
        for (int j = 0; j < 4; j++) m = fmaxf(m, acc[t][n][j]);
      m = ceilf(qmax(m));
      float l = 0;
#pragma unroll
      for (int n = 0; n < 4; n++)
#pragma unroll
        for (int j = 0; j < 4; j++) { float p = __builtin_amdgcn_exp2f(acc[t][n][j] - m); acc[t][n][j] = p; l += p; }
      unsigned o = ((rt0 + t) * 32 + kg * 2) * BLK;
      st128<SC1>(pack8(acc[t][0], acc[t][1]), rpp, lane * 16, o);
      st128<SC1>(pack8(acc[t][2], acc[t][3]), rpp, lane * 16 + 1024, o);
      rec[t] = u32x2{__float_as_uint(m), __float_as_uint(qsum(l))};
    }
    SB(); bar();
    wait_vm<0>();
    st64<SC1>(q & 1 ? rec[1] : rec[0], rs, lane < 32 ? (q & 1) * 2048 + r * 8 : OOB, (rt0 * 16 + kg) * 128);
    STAMP(1, 5);
  }
  if (idx < 24) {
    const int rt0 = rp4 * 8 + wid * 2;
    rsrc_t rx = mk_rsrc(x, 2048 * 2304), rm = mk_rsrc(ws + O_MOD, 13824), rs = mk_rsrc(ws + O_ST, 128 * 16 * 128);
    u32x2 xv[2][6], gv[6], sv[2][4];
#pragma unroll
    for (int n = 0; n < 6; n++) {
      gv[n] = ld64(rm, b * 6912 + 4608 + dp * 192 + q * 8, n * 32);
#pragma unroll
      for (int t = 0; t < 2; t++) xv[t][n] = ld64(rx, ((rt0 + t) * 16 + r) * 2304 + dp * 192 + q * 8, n * 32);
    }
    f32x4 acc[2][6]; zero(acc);
#pragma unroll 1
    for (int spin = 0; spin < SPIN_CAP; spin++) {
      bool ok = true;
#pragma unroll
      for (int t = 0; t < 2; t++)
#pragma unroll
        for (int j = 0; j < 4; j++) { sv[t][j] = ld64<SC1>(rs, (q * 4 + j) * 128 + r * 8, (rt0 + t) * 2048); ok &= sv[t][j][1] != 0; }
      if (__all(ok)) break;
      asm volatile("s_sleep 1" ::: "memory");
    }
    asm volatile("" ::: "memory"); SB();
    STAMP(1, 6);
    LDS unsigned* tab = (LDS unsigned*)(sm + L_TAB + wid * 2048);
    float inv[2];
    unsigned a0 = rt0 * 32 * BLK + lane * 16, a1 = a0 + 32 * BLK;
    f_consumer<6, 32, 8, false, R0, 147456, false, true, SC1>(mk_rsrc(ws + O_P, 128 * 32 * BLK), a0, a1, lp, ph4, acc, l0 + L_TAB + wid * 2048 + r * 64, [&] {
#pragma unroll
      for (int t = 0; t < 2; t++) {
        float mg = qmax(fmaxf(fmaxf(__uint_as_float(sv[t][0][0]), __uint_as_float(sv[t][1][0])), fmaxf(__uint_as_float(sv[t][2][0]), __uint_as_float(sv[t][3][0]))));
        float l = 0; u32x4 k;
#pragma unroll
        for (int j = 0; j < 4; j++) {
          float d = fminf(mg - __uint_as_float(sv[t][j][0]), 255.f);
          l += __uint_as_float(sv[t][j][1]) * __builtin_amdgcn_exp2f(-d);
          k[j] = (unsigned)d * 0x00800080u;
        }
        *(LDS u32x4*)(tab + t * 256 + r * 16 + ((q - ph4) & 3) * 4) = k;
        inv[t] = __builtin_amdgcn_rcpf(qsum(l));
      }
      wait_lgkm<0>();
    }, ws, 13);
    STAMP(1, 7);
    rsrc_t ro = mk_rsrc(ws + O_X1, SZ_ACT);
    u32x4 xo[2][3];
#pragma unroll
    for (int t = 0; t < 2; t++) {
#pragma unroll
      for (int n = 0; n < 6; n++) {
        acc[t][n][0] = blo(xv[t][n][0]) + blo(gv[n][0]) * (acc[t][n][0] * inv[t]);
        acc[t][n][1] = bhi(xv[t][n][0]) + bhi(gv[n][0]) * (acc[t][n][1] * inv[t]);
        acc[t][n][2] = blo(xv[t][n][1]) + blo(gv[n][1]) * (acc[t][n][2] * inv[t]);
        acc[t][n][3] = bhi(xv[t][n][1]) + bhi(gv[n][1]) * (acc[t][n][3] * inv[t]);
      }
#pragma unroll
      for (int p = 0; p < 3; p++) { xo[t][p] = pack8(acc[t][2 * p], acc[t][2 * p + 1]); st128<SC1>(xo[t][p], ro, lane * 16 + p * BLK, ((rt0 + t) * KD + dp * 3) * BLK); }
    }
    SB(); bar();
    u32x2 rec[2];
#pragma unroll
    for (int t = 0; t < 2; t++) {
      f32x2 s1 = {0, 0}, s2 = {0, 0};
#pragma unroll
      for (int p = 0; p < 3; p++)
#pragma unroll
        for (int j = 0; j < 4; j++) s1 += f32x2{blo(xo[t][p][j]), bhi(xo[t][p][j])};
      float mw = qsum(s1[0] + s1[1]) * (1.f / 96);
#pragma unroll
      for (int p = 0; p < 3; p++)
#pragma unroll
        for (int j = 0; j < 4; j++) { f32x2 d = f32x2{blo(xo[t][p][j]), bhi(xo[t][p][j])} - mw; s2 += d * d; }
      rec[t] = u32x2{__float_as_uint(mw), __float_as_uint(qsum(s2[0] + s2[1])) | 0x80000000u};
    }
    wait_vm<0>();
    st64<SC1>(q & 1 ? rec[1] : rec[0], mk_rsrc(ws + O_XS, 128 * 12 * 128), lane < 32 ? (q & 1) * 1536 + r * 8 : OOB, (rt0 * 12 + dp) * 128);
  } else if (so < 36) {
    if (wid < 2) {
      Frag<FL, 5> ft; ft.init(l0, 0, 0, lane);
      Frag<KC, 2> fw; fw.init(l0, TW, wid * 2, lane);
      f32x4 acc[2][5]; zero(acc);
      r_consumer(fw, ft, TSLOT, acc);
      STAMP(1, 7);
      rsrc_t ro = mk_rsrc(ws + O_WQC, SZ_TOK);
#pragma unroll
      for (int t = 0; t < 5; t++) st128<SC1>(pack8(acc[0][t] * QK_SCALE, acc[1][t] * QK_SCALE), ro, lane * 16, (((so & 1) * 5 + t) * KD + (so >> 1) * 2 + wid) * BLK);
      SB(); bar();
      publish(ws, FL_W + so * 2 + wid, lane);
    } else {
#pragma unroll 1
      for (int g = 0; g < 19; g++) bar();
    }
  } else if (so < 54) {
    int bb = wid >> 1, wn = wid & 1;
    Frag<FL, 5> ft; ft.init(l0, 0, bb * 5, lane);
    Frag<TR, 2> fw; fw.init(l0, TW, wn * 2, lane);
    f32x4 acc[5][2]; zero(acc);
    r_consumer(ft, fw, TSLOT, acc);
    STAMP(1, 7);
    rsrc_t ro = mk_rsrc(ws + O_VOC, SZ_TOK);
#pragma unroll
    for (int j = 0; j < 2; j++) {
      unsigned o = (bb * 72 + (so - 36) * 4 + wn * 2 + j) * 2560;
      st128<SC1>(pack8(acc[0][j], acc[1][j]), ro, lane * 16, o);
      st128<SC1>(pack8(acc[2][j], acc[3][j]), ro, lane * 16 + 1024, o);
      st64<SC1>(pack4(acc[4][j]), ro, lane * 8 + 2048, o);
    }
    SB(); bar();
    publish(ws, FL_O + (so - 36) * 4 + wid, lane);
  }
  STAMP(1, 8);
  {
    rsrc_t rx = mk_rsrc(ws + O_X1, SZ_ACT), rw = mk_rsrc(ws + O_WQC, SZ_TOK), rv = mk_rsrc(ws + O_VOC, SZ_TOK);
    u32x4 xf[9], w4[9], vf[9][2]; u32x2 vh[9], xr[9];
    const int dt0 = hf * 36 + wid * 9;
    poll(ws, FL_W + (lane < 36 ? (lane >> 1) * 4 + (lane & 1) : 0) + b * 2);
#pragma unroll
    for (int s = 0; s < 9; s++) w4[s] = ld128<SC1>(rw, lane * 16 + s * BLK, ((b * 5 + 4) * KD + wid * 9) * BLK);
    SB();
    bar();
    STAMP(1, 9);
    { int pr = (dt0 >> 1) + (lane < 5 ? lane : 0); pr -= pr > 35; poll(ws, FL_O + (pr >> 1) * 4 + b * 2 + (pr & 1)); }
#pragma unroll
    for (int n = 0; n < 9; n++) {
      unsigned o = (b * 72 + dt0 + n) * 2560;
      vf[n][0] = ld128<SC1>(rv, lane * 16, o); vf[n][1] = ld128<SC1>(rv, lane * 16 + 1024, o); vh[n] = ld64<SC1>(rv, lane * 8 + 2048, o);
    }
    SB();
    STAMP(1, 25);
    LDS char* wl = sm + wid * 36864 + lane * 16;
    f32x4 cs[5] = {};
    const u32x4 ones = {0x3f803f80u, 0x3f803f80u, 0x3f803f80u, 0x3f803f80u};
#pragma unroll
    for (int s = 0; s < 9; s++) {
#pragma unroll
      for (int t = 0; t < 4; t++) cs[t] = mfma_b(*(LDS u32x4*)(wl + s * 4096 + t * 1024), ones, cs[t]);
      cs[4] = mfma_b(w4[s], ones, cs[4]);
    }
    LDS float* csl = (LDS float*)(sm + L_TAB);
    if (r == 0) {
#pragma unroll
      for (int t = 0; t < 5; t++) *(LDS f32x4*)(csl + wid * 80 + t * 16 + q * 4) = cs[t];
    }
    SB();
    STAMP(1, 26);
    u32x2 xs[3];
    rsrc_t rs = mk_rsrc(ws + O_XS, 128 * 12 * 128);
#pragma unroll 1
    for (int spin = 0; spin < SPIN_CAP; spin++) {
      bool ok = true;
#pragma unroll
      for (int j = 0; j < 3; j++) { xs[j] = ld64<SC1>(rs, (q * 3 + j) * 128 + r * 8, rt5 * 1536); ok &= (int)xs[j][1] < 0; }
      if (__all(ok)) break;
      asm volatile("s_sleep 1" ::: "memory");
    }
    asm volatile("" ::: "memory"); SB();
    STAMP(1, 10);
#pragma unroll
    for (int s = 0; s < 9; s++) xf[s] = ld128<SC1>(rx, lane * 16 + s * BLK, (rt5 * KD + wid * 9) * BLK);
#pragma unroll
    for (int n = 0; n < 9; n++) xr[n] = ld64<SC1>(rx, lane * 16 + ((dt0 + n) & 1) * 8, (rt5 * KD + ((dt0 + n) >> 1)) * BLK);
    SB();
    float mean = qsum(__uint_as_float(xs[0][0]) + __uint_as_float(xs[1][0]) + __uint_as_float(xs[2][0])) * (1.f / 12), m2 = 0;
#pragma unroll
    for (int j = 0; j < 3; j++) { float d = __uint_as_float(xs[j][0]) - mean; m2 += __uint_as_float(xs[j][1] & 0x7fffffffu) + 96.f * d * d; }
    float rstd = rsqrtf(qsum(m2) * (1.f / DM) + 1e-5f);
    f32x4 sc[5] = {};
#pragma unroll
    for (int s = 0; s < 9; s++) {
#pragma unroll
      for (int t = 0; t < 4; t++) sc[t] = mfma_b(*(LDS u32x4*)(wl + s * 4096 + t * 1024), xf[s], sc[t]);
      sc[4] = mfma_b(w4[s], xf[s], sc[4]);
    }
#pragma unroll
    for (int t = 0; t < 5; t++) *(LDS f32x4*)(wl + t * 1024) = sc[t];
    wait_lgkm<0>(); bar();
    STAMP(1, 11);
    LDS char* rl = sm + lane * 16;
#pragma unroll
    for (int t = 0; t < 5; t++) {
      f32x4 c = (*(LDS f32x4*)(csl + t * 16 + q * 4) + *(LDS f32x4*)(csl + 80 + t * 16 + q * 4)) + (*(LDS f32x4*)(csl + 160 + t * 16 + q * 4) + *(LDS f32x4*)(csl + 240 + t * 16 + q * 4));
      f32x4 v = (*(LDS f32x4*)(rl + t * 1024) + *(LDS f32x4*)(rl + 36864 + t * 1024)) + (*(LDS f32x4*)(rl + 2 * 36864 + t * 1024) + *(LDS f32x4*)(rl + 3 * 36864 + t * 1024));
      sc[t] = (v - mean * c) * rstd;
    }
#pragma unroll
    for (int j = 1; j < 4; j++) sc[4][j] = q == 3 ? -__builtin_inff() : sc[4][j];
    float m = sc[0][0];
#pragma unroll
    for (int t = 0; t < 5; t++)
#pragma unroll
      for (int j = 0; j < 4; j++) m = fmaxf(m, sc[t][j]);
    m = qmax(m);
    float l = 0;
#pragma unroll
    for (int t = 0; t < 5; t++)
#pragma unroll
      for (int j = 0; j < 4; j++) { float p = __builtin_amdgcn_exp2f(sc[t][j] - m); sc[t][j] = p; l += p; }
    float inv = __builtin_amdgcn_rcpf(qsum(l));
    u32x4 p0 = pack8(sc[0] * inv, sc[1] * inv), p1 = pack8(sc[2] * inv, sc[3] * inv);
    u32x2 p2h = pack4(sc[4] * inv);
    u32x4 p2 = {p2h[0], p2h[1], 0u, 0u};
    STAMP(1, 27);
    rsrc_t ro = mk_rsrc(out, 2048 * 2304);
    u32x2 ov[9];
#pragma unroll
    for (int n = 0; n < 9; n++) {
      f32x4 o = {blo(xr[n][0]), bhi(xr[n][0]), blo(xr[n][1]), bhi(xr[n][1])};
      o = mfma_b(vf[n][0], p0, o);
      o = mfma_b(vf[n][1], p1, o);
      o = mfma_b(u32x4{vh[n][0], vh[n][1], 0u, 0u}, p2, o);
      ov[n] = pack4(o);
    }
    unsigned orow = (rt5 * 16 + r) * 2304;
#pragma unroll
    for (int n = 0; n < 8; n += 2) {
      auto e = __builtin_amdgcn_permlane16_swap(ov[n][0], ov[n + 1][0], false, false);
      auto f = __builtin_amdgcn_permlane16_swap(ov[n][1], ov[n + 1][1], false, false);
      st128(u32x4{e[0], f[0], e[1], f[1]}, ro, orow + (q & 1) * 32 + (q >> 1) * 16, (dt0 + n) * 32);
    }
    st64(ov[8], ro, orow + q * 8, (dt0 + 8) * 32);
  }
  ENDSTAMP(1, 12);
#endif
}

constexpr int KF_CODE = 44 * 1024;

constexpr int K0_LDS = 147456 + 9216 + 768;
extern "C" __global__ __launch_bounds__(384) void k0_pre(const char* x, const char* c, const char* y, const char* wqkv, const char* wo,
                                                        const char* wqc, const char* wkvc, const char* woc, const char* wmod, char* ws) {
#if defined(__HIP_DEVICE_COMPILE__)
  extern __shared__ __attribute__((aligned(16))) char shm[];
  STAMP(0, 0);
  int bid = blockIdx.x, xcd = bid & 7, idx = bid >> 3;
  int wid = __builtin_amdgcn_readfirstlane(threadIdx.x >> 6), lane = threadIdx.x & 63;
  int qd = xcd & 3, nr = (qd & 1) ? 4 : 5, nc = (qd & 2) ? 4 : 5, r0 = (qd & 1) ? 5 : 0, c0 = (qd & 2) ? 5 : 0;
  LDS char* sm = (LDS char*)shm;
  unsigned l0 = (unsigned)(size_t)sm;
  int r = lane & 15, q = lane >> 4;
  if (idx < nr * nc) {
    int ti = r0 + idx % nr, tj = c0 + idx / nr, mat = xcd >> 2;
    int rot = (18 * ((ti + tj) % K0PH) + K0PH / 2) / K0PH;
    constexpr int SLOT = 32768;
    if (wid >= 4) {
      RSrc<16> s;
      if (wid == 4) { s.r = mk_rsrc(wqkv, DM * 6912); s.kc(ti * 128 * 6912 + mat * 4608, 6912, lane); }
      else if (mat == 0) { s.r = mk_rsrc(wqkv, DM * 6912); s.kc(tj * 128 * 6912 + 2304, 6912, lane); }
      else { s.r = mk_rsrc(wo, DM * 2304); s.tr(tj * 256, 2304, lane); }
      LDS char* d = sm + (wid - 4) * 16384;
      r_loader<16>([&](int g, int slot) { s.issue(g, d + slot * SLOT); }, rot);
    } else {
      int wi = wid >> 1, wj = wid & 1;
      f32x4 acc[4][4]; zero(acc);
      Frag<KC, 4> fx; fx.init(l0, 0, wi * 4, lane);
      float sc = 1.f;
      if (mat == 0) { Frag<KC, 4> fy; fy.init(l0, 16384, wj * 4, lane); r_consumer(fx, fy, SLOT, acc); sc = QK_SCALE; }
      else { Frag<TR, 4> fy; fy.init(l0, 16384, wj * 4, lane); r_consumer(fx, fy, SLOT, acc); }
      STAMP(0, 2);
      rsrc_t ro = mk_rsrc(ws + O_W, 144 * KD * BLK);
      unsigned o = ((mat * 72 + tj * 8 + wj * 4) * KD + ti * 4 + wi * 2) * BLK;
#pragma unroll
      for (int j = 0; j < 4; j++) {
        st128<K0AUX>(pack8(acc[0][j] * sc, acc[1][j] * sc), ro, lane * 16, o + j * KD * BLK);
        st128<K0AUX>(pack8(acc[2][j] * sc, acc[3][j] * sc), ro, lane * 16 + 1024, o + j * KD * BLK);
      }
      ENDSTAMP(0, 3);
    }
    return;
  }
  int o = (xcd >> 2) * 47 + (qd == 0 ? 0 : qd == 1 ? 7 : qd == 2 ? 19 : 31) + idx - nr * nc;
  if (o < 72 && !(o & 1)) {
    constexpr int SLOT = 28 * 1024, WOFF = 20 * 1024;
    int pn = o >> 1, rot = (18 * (pn % K0PH) + K0PH / 2) / K0PH;
    if (wid == 4) {
      RSrc<20> s; s.r = mk_rsrc(y, 154 * 2304); s.kstep = 128;
#pragma unroll
      for (int p = 0; p < 20; p++) {
        int row = 8 * p + (lane >> 3), b = row >= 80, t = row - 80 * b;
        s.v[p] = t < 77 ? (b * 77 + t) * 2304 + (((lane & 7) ^ ((row >> 1) & 7)) << 4) - (p & 3) * 1024 : OOB;
      }
      r_loader<20>([&](int g, int slot) { s.issue(g, sm + slot * SLOT); }, rot);
    } else if (wid == 5) {
      RSrc<8> s; s.r = mk_rsrc(wkvc, DM * 4608); s.tr(pn * 128, 4608, lane);
#pragma unroll 1
      for (int g = 0; g < 18; g++) s.issue(g, sm + 3 * SLOT + WOFF);
      r_loader<8>([&](int g, int slot) { s.issue(g, sm + slot * SLOT + WOFF); }, rot);
    } else {
      int b = wid >> 1, wn = wid & 1;
      Frag<KC, 5> ft; ft.init(l0, 0, b * 5, lane);
      Frag<TR, 2> fw; fw.init(l0, WOFF, wn * 2, lane);
      f32x4 acc[2][5]; zero(acc);
      r_consumer(fw, ft, SLOT, acc);
      STAMP(0, 2);
      rsrc_t ro = mk_rsrc(ws + O_KC, 2 * SZ_TOK);
      int kb = pn * 2 + wn, sel = kb >= KD;
      unsigned oo = sel * SZ_TOK + (kb - sel * KD) * BLK, vo = ((q >> 1) * 16 + r) * 16 + (q & 1) * 8;
#pragma unroll
      for (int t = 0; t < 5; t++) {
        st64(pack4(acc[0][t]), ro, vo, oo + (b * 5 + t) * KD * BLK);
        st64(pack4(acc[1][t]), ro, vo + 512, oo + (b * 5 + t) * KD * BLK);
      }
      ENDSTAMP(0, 3);
    }
    return;
  }
  int ax = o < 72 ? o >> 1 : o - 36;
  int pan = ax < 36 ? ax : ax + 14;
  GAS u64* flg = (GAS u64*)(ws + O_FLAG);
  u32x3 cv;
  { u32x4 dc = mk_desc(c, 2 * 2304); unsigned vo = (lane >> 5) * 2304 + wid * 384 + (lane & 31) * 12;
    asm volatile("buffer_load_dwordx3 %0, %1, %2, 0 offen" : "=v"(cv) : "v"(vo), "s"(dc) : "memory"); }
  auto wm_issue = [&]() {
    RSrc<8> s; s.tr(pan * 128 + wid * 192 * 13824, 13824, lane);
    u32x4 dw = mk_desc(wmod, DM * 13824);
#pragma unroll
    for (int g = 0; g < 3; g++) {
      dma4_asm(dw, l0 + wid * 24576 + g * 8192, s.v[0], s.v[1], s.v[2], s.v[3], g * 64 * 13824);
      dma4_asm(dw, l0 + wid * 24576 + g * 8192 + 4096, s.v[4], s.v[5], s.v[6], s.v[7], g * 64 * 13824);
    }
  };
  LDS float* red = (LDS float*)(sm + 147456);
  LDS unsigned* sil = (LDS unsigned*)(sm + 147456 + 3072);
  LDS float* tabf = (LDS float*)(sm + 147456);
  LDS f32x2* st = (LDS f32x2*)(sm + 147456 + 9216);
  auto wm_silu = [&](u32x4 (&cf)[6]) {
#pragma unroll
    for (int j = 0; j < 3; j++) {
      float a = blo(cv[j]), b = bhi(cv[j]);
      sil[wid * 192 + lane * 3 + j] = pk2(a * __builtin_amdgcn_rcpf(1.f + __expf(-a)), b * __builtin_amdgcn_rcpf(1.f + __expf(-b)));
    }
    wait_lgkm<0>();
    unsigned sa = l0 + 147456 + 3072 + wid * 768 + (r & 1) * 384 + q * 16;
    sfor<0, 6>([&](auto i) { dsr128<decltype(i)::value * 64>(cf[decltype(i)::value], sa); });
  };
  auto wm_mma = [&](u32x4 (&cf)[6]) {
    Frag<TR, 4> fw; fw.init(l0, wid * 24576, 0, lane);
    f32x4 acc[1][4]; zero(acc); pin2(acc);
    sfor<0, 3>([&](auto i) {
      constexpr int g = decltype(i)::value;
      u32x4 w[2][4];
      sfor<0, 4>([&](auto jj) { fw.template read<0, decltype(jj)::value>(w[0][decltype(jj)::value], g * 8192); fw.template read<1, decltype(jj)::value>(w[1][decltype(jj)::value], g * 8192); });
      wait_lgkm<0>();
#pragma unroll
      for (int h = 0; h < 2; h++) {
        pin(cf[2 * g + h]);
#pragma unroll
        for (int j = 0; j < 4; j++) { pin(w[h][j]); mfma(acc[0][j], w[h][j], cf[2 * g + h]); }
      }
    });
    mfma_done(); pin2(acc);
    if (r < 2) {
#pragma unroll
      for (int j = 0; j < 4; j++) *(LDS f32x4*)(red + wid * 128 + r * 64 + j * 16 + q * 4) = acc[0][j];
    }
    wait_lgkm<0>(); bar();
    if (wid == 0) {
      f32x4 a = {}, b = {};
#pragma unroll
      for (int w = 0; w < 6; w++) { a += *(LDS f32x4*)(red + w * 128 + r * 8); b += *(LDS f32x4*)(red + w * 128 + r * 8 + 4); }
      st128<SC1>(pack8(a, b), mk_rsrc(ws + O_MOD, 13824), lane < 16 ? (lane >> 3) * 6912 + pan * 128 + (lane & 7) * 16 : OOB, 0);
    }
  };
  u64 ep;
  asm volatile("global_load_dwordx2 %0, %1, %2 sc1" : "=v"(ep) : "v"(0u), "s"(flg) : "memory");
  if (ax < 40) wm_issue();
  SB();
  int hb = ax - 14, hbat = hb >= 22, hj = (hb - 22 * hbat) * 3 + (wid >> 1), ht = hbat * 64 + hj, kh = wid & 1;
  bool hval = hb >= 0 && hj < 64;
  u32x4 xv[18];
  { rsrc_t rx = mk_rsrc(x, 2048 * 2304);
    unsigned vo = hval ? (ht * 16 + r) * 2304 + kh * 1152 + q * 16 : OOB;
#pragma unroll
    for (int s = 0; s < 18; s++) xv[s] = ld128(rx, vo, s * 64); }
  SB();
  u32x4 cf[6];
  if (ax < 40) {
    asm volatile("s_waitcnt vmcnt(42)" : "+v"(cv), "+v"(ep)::"memory");
    wm_silu(cf);
    wait_vm<18>();
    wm_mma(cf);
    if (wid == 0) wait_vm<0>();
  } else asm volatile("s_waitcnt vmcnt(18)" : "+v"(cv), "+v"(ep)::"memory");
  u64 tag = mix64(ep);
  if (wid == 0 && lane == 0) __hip_atomic_store(flg + 8 + ax, tag, __ATOMIC_RELAXED, __HIP_MEMORY_SCOPE_AGENT);
  STAMP(0, 1);
  if (hb >= 0) {
    f32x2 xf[18][4], s1 = {0, 0};
#pragma unroll
    for (int s = 0; s < 18; s++)
#pragma unroll
      for (int j = 0; j < 4; j++) { xf[s][j] = f32x2{blo(xv[s][j]), bhi(xv[s][j])}; s1 += xf[s][j]; }
    float mw = qsum(s1[0] + s1[1]) * (1.f / 576);
    f32x2 s2 = {0, 0};
#pragma unroll
    for (int s = 0; s < 18; s++)
#pragma unroll
      for (int j = 0; j < 4; j++) { xf[s][j] -= mw; s2 += xf[s][j] * xf[s][j]; }
    float m2 = qsum(s2[0] + s2[1]);
    if (q == 0) st[wid * 16 + r] = f32x2{mw, m2};
    if (wid == 0) {
      int n = ax == 57 ? 58 : 36;
    #pragma unroll 1
  for (int spin = 0; spin < SPIN_CAP; spin++) {
        u64 f = __hip_atomic_load(flg + 8 + (lane < n ? lane : 0), __ATOMIC_RELAXED, __HIP_MEMORY_SCOPE_AGENT);
        if (__all(f == tag)) break;
        __builtin_amdgcn_s_sleep(1);
      }
      if (ax == 57 && lane == 0) __hip_atomic_store(flg, tag, __ATOMIC_RELAXED, __HIP_MEMORY_SCOPE_AGENT);
    }
    STAMP(0, 2);
    wait_lgkm<0>(); bar();
    int t = threadIdx.x;
    u32x4 mv = ld128<SC1>(mk_rsrc(ws + O_MOD, 13824), t < 288 ? t * 16 : OOB, hbat * 6912);
    f32x2 so = st[(wid ^ 1) * 16 + r];
    float dm = 0.5f * (so[0] - mw), rstd = rsqrtf((m2 + so[1] + 2 * 576 * dm * dm) * (1.f / DM) + 1e-5f);
#pragma unroll
    for (int s = 0; s < 18; s++)
#pragma unroll
      for (int j = 0; j < 4; j++) xf[s][j] = (xf[s][j] - dm) * rstd;
    if (t < 288) {
      float one = t >= 144 ? 1.f : 0.f;
      *(LDS f32x4*)(tabf + t * 8) = f32x4{blo(mv[0]), bhi(mv[0]), blo(mv[1]), bhi(mv[1])} + one;
      *(LDS f32x4*)(tabf + t * 8 + 4) = f32x4{blo(mv[2]), bhi(mv[2]), blo(mv[3]), bhi(mv[3])} + one;
    }
    wait_lgkm<0>(); bar();
    rsrc_t rh = mk_rsrc(ws + O_H, SZ_ACT);
    unsigned ho = hval ? (ht * KD + kh * 18) * BLK + ((((q & 1) << 1) | (q >> 1)) * 16 + r) * 16 : OOB;
    LDS float* tp = tabf + kh * 576 + q * 8;
#pragma unroll
    for (int s = 0; s < 18; s++) {
      f32x4 f0 = *(LDS f32x4*)(tp + s * 32), f1 = *(LDS f32x4*)(tp + s * 32 + 4), a0 = *(LDS f32x4*)(tp + 1152 + s * 32), a1 = *(LDS f32x4*)(tp + 1152 + s * 32 + 4);
      f32x2 h0 = xf[s][0] * a0.xy + f0.xy, h1 = xf[s][1] * a0.zw + f0.zw, h2 = xf[s][2] * a1.xy + f1.xy, h3 = xf[s][3] * a1.zw + f1.zw;
      auto e = __builtin_amdgcn_permlane32_swap(pk2(h0[0], h0[1]), pk2(h2[0], h2[1]), false, false);
      auto f = __builtin_amdgcn_permlane32_swap(pk2(h1[0], h1[1]), pk2(h3[0], h3[1]), false, false);
      st128<K0AUX>(u32x4{e[0], f[0], e[1], f[1]}, rh, ho, s * BLK);
    }
    STAMP(0, 4);
  } else {
    pan = 36 + ax;
    wait_lgkm<0>(); bar();
    wm_issue();
    wait_vm<0>();
    wm_mma(cf);
  }
  { rsrc_t rz = mk_rsrc(ws + O_FL, CLR_BYTES);
    unsigned t = (ax * 384 + threadIdx.x) * 16;
    st128(u32x4{0, 0, 0, 0}, rz, t, 0); st128(u32x4{0, 0, 0, 0}, rz, t + 58 * 384 * 16, 0); }
  wait_lgkm<0>(); bar();
  {
    u64 pc;
    asm volatile("s_getpc_b64 s[40:41]\n\ts_add_u32 s40, s40, kf_main@rel32@lo+4\n\ts_addc_u32 s41, s41, kf_main@rel32@hi+12\n\ts_mov_b64 %0, s[40:41]" : "=s"(pc)::"s40", "s41", "scc");
    unsigned v = lane * 16;
    u32x4 dcode = mk_desc((const void*)pc, KF_CODE);
    if (idx - nr * nc < 2) for (int p = wid * 4096; p < KF_CODE; p += 6 * 4096) dma4_asm(dcode, l0 + wid * 4096, v, v, v, v, p);
    int first = (ax * 6 + wid) * 16;
    u32x4 d1 = mk_desc(wqc, DM * 2304), d2 = mk_desc(woc, DM * 2304);
    for (int p = first; p < first + 16; p += 4) {
      if (p < 2592) dma4_asm(d1, l0 + wid * 4096, v, v, v, v, p * 1024); else dma4_asm(d2, l0 + wid * 4096, v, v, v, v, (p - 2592) * 1024);
    }
  }
  ENDSTAMP(0, 3);
#endif
}

extern "C" __global__ void amp_k(char* ws) {
#if defined(__HIP_DEVICE_COMPILE__)
  __shared__ u64 red[256]; __shared__ unsigned cnt[256];
  GAS u64* p = (GAS u64*)(ws + O_PROF);
  int b = threadIdx.x;
  red[b] = p[b * 32]; __syncthreads();
  u64 t0 = ~0ull; for (int i = 0; i < 256; i++) t0 = red[i] < t0 ? red[i] : t0;
  __syncthreads();
  u64 v = p[(0 * 256 + b) * 32 + 1] - t0; bool ok = v < 30000;
  red[b] = ok ? v : 0; cnt[b] = ok; __syncthreads();
  u64 m = 0, sum = 0; unsigned n = 0; for (int i = 0; i < 256; i++) { m = red[i] > m ? red[i] : m; sum += red[i]; n += cnt[i]; }
  u64 dt = m * 100;
  u64 t1 = __builtin_amdgcn_s_memrealtime();
  while (__builtin_amdgcn_s_memrealtime() - t1 < dt) __builtin_amdgcn_s_sleep(32);
  for (int i = 0; i < 64; i++) p[b * 64 + i] = 0;
#endif
}
extern "C" void kernel_launch(void* const* d_in, const int* in_sizes, int n_in, void* d_out, int out_size, void* d_ws, size_t ws_size,
                              hipStream_t stream) {
  const char *x = (const char*)d_in[0], *c = (const char*)d_in[1], *y = (const char*)d_in[2], *wqkv = (const char*)d_in[3], *wo = (const char*)d_in[4],
             *wqc = (const char*)d_in[5], *wkvc = (const char*)d_in[6], *woc = (const char*)d_in[7], *wmod = (const char*)d_in[8];
  char* ws = (char*)d_ws;
  (void)hipFuncSetAttribute((const void*)k0_pre, hipFuncAttributeMaxDynamicSharedMemorySize, K0_LDS);
  (void)hipFuncSetAttribute((const void*)kf_main, hipFuncAttributeMaxDynamicSharedMemorySize, KF_LDS);
  k0_pre<<<256, 384, K0_LDS, stream>>>(x, c, y, wqkv, wo, wqc, wkvc, woc, wmod, ws);
  kf_main<<<256, 320, KF_LDS, stream>>>(x, wqc, woc, ws, (char*)d_out);
  amp_k<<<1, 256, 0, stream>>>(ws);
}
```

```cpp
#define PROF 1
#define K0PH 2
#include <hip/hip_runtime.h>
#include <hip/hip_bf16.h>
#include <type_traits>

typedef __attribute__((ext_vector_type(8))) __bf16 bf16x8;
typedef __attribute__((ext_vector_type(2))) __bf16 bf16x2;
typedef __attribute__((ext_vector_type(4))) float f32x4;
typedef __attribute__((ext_vector_type(2))) float f32x2;
typedef __attribute__((ext_vector_type(4))) unsigned u32x4;
typedef __attribute__((ext_vector_type(3))) unsigned u32x3;
typedef __attribute__((ext_vector_type(2))) unsigned u32x2;
typedef unsigned long long u64;
#define LDS __attribute__((address_space(3)))
#define GAS __attribute__((address_space(1)))
#define DI __device__ __forceinline__

constexpr int DM = 1152;
constexpr int KD = 36;
constexpr int BLK = 1024;
constexpr size_t O_FLAG = 0;
constexpr size_t O_MOD = 4096;
constexpr size_t O_MG = 12288;
constexpr size_t O_FL = 65536;
constexpr int FL_T = 0, FL_V = 512, FL_W = 1024, FL_O = 1096, FL_N = 1168;
constexpr size_t O_ST = O_FL + 1280 * 128;
constexpr size_t O_XS = O_ST + 128 * 16 * 128;
constexpr size_t CLR_BYTES = 1280 * 128 + 128 * 16 * 128 + 128 * 12 * 128;
constexpr size_t O_H = O_FL + CLR_BYTES;
constexpr size_t SZ_ACT = 128ull * 36 * 1024;
constexpr size_t O_W = O_H + SZ_ACT;
constexpr size_t O_T = O_W + 144ull * 36 * 1024;
constexpr size_t O_V = O_T + SZ_ACT;
constexpr size_t O_P = O_V + SZ_ACT;
constexpr size_t O_X1 = O_P + 128ull * 32 * 1024;
constexpr size_t SZ_TOK = 10ull * 36 * 1024;
constexpr size_t O_KC = O_X1 + SZ_ACT;
constexpr size_t O_VC = O_KC + SZ_TOK;
constexpr size_t O_WQC = O_VC + SZ_TOK;
constexpr size_t O_VOC = O_WQC + SZ_TOK;

constexpr size_t O_PROF = 200ull << 20;
#define STAMP(k, i) do { if (__builtin_amdgcn_readfirstlane(threadIdx.x) < 64) { ((GAS u64*)(ws + O_PROF))[((k) * 256 + blockIdx.x) * 32 + (i)] = __builtin_amdgcn_s_memrealtime(); } } while (0)
#define STAMPL(i) do { ((GAS u64*)(ws + O_PROF))[(256 + blockIdx.x) * 32 + (i)] = __builtin_amdgcn_s_memrealtime(); } while (0)
#define ENDSTAMP(k, i) do { wait_vm<0>(); STAMP(k, i); } while (0)
#define VTHR 63
#define PH2 0
#define VDELAY 24
#define TDELAY 12
#define PDELAY 16
#define PNAP 6
#define VSPLIT 4
#define WPF 1
#define KFW 1
#define K0AUX 16

constexpr float QK_SCALE = 1.4426950408889634f * 0.029462782549439476f;

#if defined(__HIP_DEVICE_COMPILE__)
typedef __amdgpu_buffer_rsrc_t rsrc_t;
DI rsrc_t mk_rsrc(const void* p, unsigned bytes) { return __builtin_amdgcn_make_buffer_rsrc((void*)p, 0, bytes, 0x00020000); }
DI u32x4 mk_desc(const void* p, unsigned bytes) { u64 a = (u64)p; return u32x4{(unsigned)a, (unsigned)(a >> 32) & 0xffffu, bytes, 0x00020000u}; }
DI unsigned pk2(float a, float b) { f32x2 p = {a, b}; return __builtin_bit_cast(unsigned, __builtin_convertvector(p, bf16x2)); }
DI float blo(unsigned u) { return __uint_as_float(u << 16); }
DI float bhi(unsigned u) { return __uint_as_float(u & 0xffff0000u); }
template <int N> DI void wait_vm() { asm volatile("s_waitcnt vmcnt(%0)" ::"n"(N > 63 ? 63 : N) : "memory"); }
template <int N> DI void wait_lgkm() { asm volatile("s_waitcnt lgkmcnt(%0)" ::"n"(N > 15 ? 15 : N) : "memory"); }
DI void SB() { __builtin_amdgcn_sched_barrier(0); }
DI void bar() { asm volatile("s_barrier" ::: "memory"); }
template <int I, int N, class F> DI void sfor(F&& f) { if constexpr (I < N) { f(std::integral_constant<int, I>{}); sfor<I + 1, N>(f); } }
template <int V> using ic = std::integral_constant<int, V>;
DI void mfma(f32x4& c, const u32x4& x, const u32x4& y) { asm volatile("v_mfma_f32_16x16x32_bf16 %0, %1, %2, %0" : "+v"(c) : "v"(x), "v"(y)); }
DI f32x4 mfma_b(const u32x4& x, const u32x4& y, const f32x4& c) {
  return __builtin_amdgcn_mfma_f32_16x16x32_bf16(__builtin_bit_cast(bf16x8, x), __builtin_bit_cast(bf16x8, y), c, 0, 0, 0);
}
template <class T> DI void pin(T& x) { asm volatile("" : "+v"(x)); }
template <int OFF> DI void dsr128(u32x4& d, unsigned a) { asm volatile("ds_read_b128 %0, %1 offset:%2" : "=v"(d) : "v"(a), "n"(OFF)); }
template <int OFF> DI void dsr_tr(u32x2& d, unsigned a) { asm volatile("ds_read_b64_tr_b16 %0, %1 offset:%2" : "=v"(d) : "v"(a), "n"(OFF)); }
constexpr int SC1 = 16;
template <int AUX = 0> DI u32x4 ld128(rsrc_t r, unsigned v, unsigned s) { return __builtin_amdgcn_raw_buffer_load_b128(r, v, s, AUX); }
template <int AUX = 0> DI u32x2 ld64(rsrc_t r, unsigned v, unsigned s) { return __builtin_amdgcn_raw_buffer_load_b64(r, v, s, AUX); }
template <int AUX = 0> DI void st128(u32x4 d, rsrc_t r, unsigned v, unsigned s) {
  __builtin_amdgcn_raw_buffer_store_b128(d, r, v, s, AUX); SB(); asm volatile("s_nop 1"); SB();
}
template <int AUX = 0> DI void st128b(u32x4 d, rsrc_t r, unsigned v, unsigned s) { __builtin_amdgcn_raw_buffer_store_b128(d, r, v, s, AUX); }
DI void st_end() { SB(); asm volatile("s_nop 1"); SB(); }
template <int N> DI void pin_all(u32x4 (&a)[N]) {
#pragma unroll
  for (int i = 0; i < N; i++) pin(a[i]);
  SB();
}
template <int AUX = 0> DI void st64(u32x2 d, rsrc_t r, unsigned v, unsigned s) { __builtin_amdgcn_raw_buffer_store_b64(d, r, v, s, AUX); }
template <int IMM, int AUX = 0> DI void dma16(rsrc_t r, LDS char* d, unsigned v, unsigned s) { __builtin_amdgcn_raw_ptr_buffer_load_lds(r, d, 16, v, s, IMM, AUX); }
DI void dma4_asm(u32x4 desc, unsigned lds, unsigned v0, unsigned v1, unsigned v2, unsigned v3, unsigned s) {
  unsigned keep;
  asm volatile("s_mov_b32 %0, m0\n\ts_mov_b32 m0, %2\n\ts_nop 0\n\t"
               "buffer_load_dwordx4 %3, %1, %7 offen lds\n\tbuffer_load_dwordx4 %4, %1, %7 offen offset:1024 lds\n\t"
               "buffer_load_dwordx4 %5, %1, %7 offen offset:2048 lds\n\tbuffer_load_dwordx4 %6, %1, %7 offen offset:3072 lds\n\ts_mov_b32 m0, %0"
               : "=&s"(keep) : "s"(desc), "s"(lds), "v"(v0), "v"(v1), "v"(v2), "v"(v3), "s"(s) : "memory");
}
#define CODE_WARM(SYM, BYTES, PIECE, LDSADDR) do { unsigned keep_; u64 pc_; \
  asm volatile("s_getpc_b64 s[40:41]\n\ts_add_u32 s40, s40, " #SYM "@rel32@lo+4\n\ts_addc_u32 s41, s41, " #SYM "@rel32@hi+12\n\ts_mov_b64 %0, s[40:41]" : "=s"(pc_)::"s40", "s41", "scc"); \
  asm volatile("s_mov_b32 %0, m0\n\ts_mov_b32 m0, %2\n\ts_nop 0\n\tbuffer_load_dwordx4 %3, %1, %4 offen lds\n\tbuffer_load_dwordx4 %3, %1, %5 offen lds\n\ts_mov_b32 m0, %0" \
               : "=&s"(keep_) : "s"(mk_desc((const void*)pc_, BYTES)), "s"(LDSADDR), "v"(lane * 16), "s"((PIECE) * 1024), "s"((PIECE) * 1024 + 32768) : "memory"); } while (0)
DI u32x4 pack8(const f32x4& a, const f32x4& b) { return u32x4{pk2(a[0], a[1]), pk2(a[2], a[3]), pk2(b[0], b[1]), pk2(b[2], b[3])}; }
DI u32x2 pack4(const f32x4& a) { return u32x2{pk2(a[0], a[1]), pk2(a[2], a[3])}; }
DI void mfma_done() { asm volatile("s_nop 15" ::: "memory"); }
DI float qmax(float x) {
  auto a = __builtin_amdgcn_permlane32_swap(__float_as_uint(x), __float_as_uint(x), false, false);
  x = fmaxf(__uint_as_float(a[0]), __uint_as_float(a[1]));
  auto b = __builtin_amdgcn_permlane16_swap(__float_as_uint(x), __float_as_uint(x), false, false);
  return fmaxf(__uint_as_float(b[0]), __uint_as_float(b[1]));
}
DI float qsum(float x) {
  auto a = __builtin_amdgcn_permlane32_swap(__float_as_uint(x), __float_as_uint(x), false, false);
  x = __uint_as_float(a[0]) + __uint_as_float(a[1]);
  auto b = __builtin_amdgcn_permlane16_swap(__float_as_uint(x), __float_as_uint(x), false, false);
  return __uint_as_float(b[0]) + __uint_as_float(b[1]);
}
constexpr unsigned OOB = 0x7ff00000u;
constexpr int SPIN_CAP = 1 << 18;
template <int NX, int NY> DI void zero(f32x4 (&a)[NX][NY]) {
#pragma unroll
  for (int i = 0; i < NX; i++)
#pragma unroll
    for (int j = 0; j < NY; j++) a[i][j] = f32x4{0, 0, 0, 0};
}
template <int NX, int NY> DI void pin2(f32x4 (&a)[NX][NY]) {
#pragma unroll
  for (int i = 0; i < NX; i++)
#pragma unroll
    for (int j = 0; j < NY; j++) pin(a[i][j]);
}

DI void publish(char* ws, int flag, int lane) {
  wait_vm<0>();
  __builtin_amdgcn_raw_buffer_store_b32(1u, mk_rsrc(ws + O_FL, FL_N * 128), lane == 0 ? 0u : OOB, flag * 128, SC1);
}
template <int NAP = 6> DI void poll(const char* ws, int flag_of_lane) {
  GAS const unsigned* p = (GAS const unsigned*)(ws + O_FL) + flag_of_lane * 32;
#pragma unroll 1
  for (int spin = 0; spin < SPIN_CAP; spin++) {
    unsigned v = __hip_atomic_load(p, __ATOMIC_RELAXED, __HIP_MEMORY_SCOPE_AGENT);
    if (__all(v == 1u)) break;
    __builtin_amdgcn_s_sleep(NAP);
  }
  asm volatile("" ::: "memory"); SB();
}

DI bool ready(const char* ws, int flag_of_lane) {
  unsigned v = __hip_atomic_load((GAS const unsigned*)(ws + O_FL) + flag_of_lane * 32, __ATOMIC_RELAXED, __HIP_MEMORY_SCOPE_AGENT);
  bool ok = __all(v == 1u);
  asm volatile("" ::: "memory"); SB();
  return ok;
}

template <int NT, int KS, int D, bool PERG, int START, int WRAP, bool SWAP, bool SCALE, int AUX, class PRE>
DI void f_consumer(rsrc_t ra, unsigned a0, unsigned a1, unsigned lp, int pb, f32x4 (&acc)[2][NT], unsigned tab, PRE&& pre, char* ws = nullptr, int sb = -1) {
  constexpr int U = D / 2, NB = KS / D, SLOT = NT * 2048, REG = U * SLOT;
  static_assert(KS % D == 0, "");
  u32x4 a[D][2];
  unsigned sa = pb * D * BLK;
#pragma unroll
  for (int d = 0; d < D - 1; d++) { a[d][0] = ld128<AUX>(ra, a0, sa + d * BLK); SB(); a[d][1] = ld128<AUX>(ra, a1, sa + d * BLK); SB(); }
  u32x4 b0[NT], b1[NT], sh[2], shn[2];
  auto scale = [&](u32x4& v, int j, unsigned s) { asm volatile("v_pk_sub_u16 %0, %0, %1 clamp" : "+v"(v[j]) : "v"(s)); };
  pre();
  if constexpr (SCALE) {
    dsr128<0>(sh[0], tab); dsr128<1024>(sh[1], tab);
    wait_lgkm<0>(); pin(sh[0]); pin(sh[1]);
#pragma unroll
    for (int j = 0; j < 4; j++) { scale(a[0][0], j, sh[0][0]); scale(a[0][1], j, sh[1][0]); }
  }
  auto half = [&](u32x4 (&av)[2], u32x4 (&bc)[NT], u32x4 (&bn)[NT], unsigned addr, auto off, u32x4 (&ar)[2], unsigned so, auto nx,
                  u32x4 (&an)[2], unsigned s0, unsigned s1) {
    sfor<0, NT>([&](auto i) {
      constexpr int I = decltype(i)::value, WS = NT == 4 ? 2 : 3;
      if constexpr (I % WS == 0) { constexpr int need = (I + WS < NT ? I + WS : NT); wait_lgkm<NT - need + I + decltype(nx)::value>(); }
      if constexpr (SWAP) mfma(acc[0][I], av[0], bc[I]); else mfma(acc[0][I], bc[I], av[0]);
      dsr128<I * 2048 + decltype(off)::value>(bn[I], addr);
      if constexpr (SWAP) mfma(acc[1][I], av[1], bc[I]); else mfma(acc[1][I], bc[I], av[1]);
      if constexpr (I == 0) { SB(); ar[0] = ld128<AUX>(ra, a0, so); SB(); }
      if constexpr (I == 2) { SB(); ar[1] = ld128<AUX>(ra, a1, so); SB(); }
      if constexpr (SCALE && I >= 1 && I <= 4) { scale(an[(I - 1) >> 1], ((I - 1) & 1) * 2, I < 3 ? s0 : s1); scale(an[(I - 1) >> 1], ((I - 1) & 1) * 2 + 1, I < 3 ? s0 : s1); }
    });
  };
  pin2(acc);
  SB();
  bar();
  if (sb >= 0) STAMP(1, sb);
  unsigned cur = lp + START; int reg = START;
  sfor<0, NT>([&](auto i) { dsr128<decltype(i)::value * 2048>(b0[decltype(i)::value], cur); });
#pragma unroll 1
  for (int it = 0; it < NB; it++) {
    bool last = it == NB - 1;
    pb = pb + 1 == NB ? 0 : pb + 1;
    unsigned sn = last ? OOB : pb * D * BLK;
    reg = reg + REG == WRAP ? 0 : reg + REG;
    unsigned nxt = lp + reg;
    if constexpr (SCALE) { tab += 16; dsr128<0>(shn[0], tab); dsr128<1024>(shn[1], tab); }
    sfor<0, U>([&](auto uu) {
      constexpr int u = decltype(uu)::value;
      half(a[2 * u], b0, b1, cur, ic<u * SLOT + 1024>{}, a[(2 * u + D - 1) % D], u == 0 ? sa + (D - 1) * BLK : sn + (2 * u - 1) * BLK,
           ic<(SCALE && u == 0) ? 2 : 0>{}, a[2 * u + 1], sh[0][u & 3], sh[1][u & 3]);
      if constexpr (u < U - 1) {
        if constexpr (PERG) bar();
        half(a[2 * u + 1], b1, b0, cur, ic<(u + 1) * SLOT>{}, a[2 * u], sn + 2 * u * BLK, ic<0>{}, a[2 * u + 2], sh[0][(u + 1) & 3], sh[1][(u + 1) & 3]);
      } else {
        if (!last) bar();
        if (sb >= 0 && it < 3) STAMP(1, sb + 1 + it);
        if constexpr (SCALE) { pin(shn[0]); pin(shn[1]); }
        half(a[2 * u + 1], b1, b0, nxt, ic<0>{}, a[2 * u], sn + 2 * u * BLK, ic<0>{}, a[0], shn[0][0], shn[1][0]);
      }
    });
    if constexpr (SCALE) { sh[0] = shn[0]; sh[1] = shn[1]; }
    cur = nxt; sa = sn;
  }
  wait_lgkm<0>();
  mfma_done();
  pin2(acc);
}
template <int NT, int KS, int AUX> struct FSrc {
  rsrc_t r; unsigned v[NT]; int rot;
  DI void init(rsrc_t rb, unsigned boff, int rot_, int lane) {
    r = rb; rot = rot_;
#pragma unroll
    for (int n = 0; n < NT; n++) v[n] = boff + lane * 16 + n * KS * BLK - (n & 1) * 2048;
  }
  DI void issue(int g, LDS char* dst) const {
    int gg = g + rot; gg = gg >= KS / 2 ? gg - KS / 2 : gg;
    unsigned so = gg * 2048;
    sfor<0, NT>([&](auto i) {
      constexpr int n = decltype(i)::value;
      dma16<(n & 1) * 2048, AUX>(r, dst + (n >> 1) * 4096, v[n], so);
      dma16<(n & 1) * 2048 + 1024, AUX>(r, dst + (n >> 1) * 4096, v[n], so);
    });
  }
};

enum { KC = 0, TR = 1, FL = 2 };
template <int NP> struct RSrc {
  rsrc_t r; unsigned v[NP]; unsigned kstep;
  DI void kc(unsigned base, unsigned stride, int lane) {
#pragma unroll
    for (int p = 0; p < NP; p++) { int row = 8 * p + (lane >> 3); v[p] = base + row * stride + (((lane & 7) ^ ((row >> 1) & 7)) << 4) - (p & 3) * 1024; }
    kstep = 128;
  }
  DI void tr(unsigned base, unsigned stride, int lane) {
#pragma unroll
    for (int p = 0; p < NP; p++) {
      int row = 8 * (p & 7) + (lane >> 3), f = ((row >> 1) & 1) | (((row >> 3) & 1) << 1);
      v[p] = base + (p >> 3) * 128 + row * stride + (((lane & 7) ^ (f << 1)) << 4) - (p & 3) * 1024;
    }
    kstep = 64 * stride;
  }
  DI void fl(unsigned base, int lane) {
#pragma unroll
    for (int p = 0; p < NP; p++) v[p] = base + (p >> 1) * KD * BLK + (p & 1) * BLK + lane * 16 - (p & 3) * 1024;
    kstep = 2048;
  }
  DI void issue(int gg, LDS char* dst) const {
    unsigned so = gg * kstep;
    sfor<0, NP>([&](auto i) { constexpr int p = decltype(i)::value; dma16<(p & 3) * 1024>(r, dst + (p >> 2) * 4096, v[p], so); });
  }
};
template <int NP, class F> DI void r_loader(F&& iss, int rot) {
  constexpr int G = 18;
  auto issue = [&](int g, int slot) { int gg = g + rot; iss(gg >= G ? gg - G : gg, slot); };
  issue(0, 0); issue(1, 1);
  wait_vm<NP>(); bar();
  issue(2, 2); issue(3, 3);
  int slot = 0;
#pragma unroll 1
  for (int g = 1; g < G - 3; g++) {
    wait_vm<2 * NP>(); bar();
    issue(g + 3, slot); slot = (slot + 1) & 3;
  }
  wait_vm<2 * NP>(); bar();
  wait_vm<NP>(); bar();
  wait_vm<0>(); bar();
}
template <int KIND, int NTL> struct Frag {
  unsigned base[KIND == TR ? NTL : (KIND == KC ? 2 : 1)];
  DI void init(unsigned lds0, int ioff, int t0, int lane) {
    int r = lane & 15, q = lane >> 4;
    if constexpr (KIND == KC) {
      unsigned b = lds0 + ioff + (t0 * 16 + r) * 128;
      base[0] = b + ((q ^ ((r >> 1) & 7)) << 4); base[1] = b + (((4 + q) ^ ((r >> 1) & 7)) << 4);
    } else if constexpr (KIND == TR) {
      int qq = r >> 2, p = r & 3, f = ((qq >> 1) & 1) | ((q & 1) << 1);
#pragma unroll
      for (int c = 0; c < NTL; c++) { int t = t0 + c; base[c] = lds0 + ioff + (t >> 2) * 8192 + (8 * q + qq) * 128 + (((t & 3) ^ f) << 5) + p * 8; }
    } else base[0] = lds0 + ioff + t0 * 2048 + lane * 16;
  }
  template <int H, int I> DI void read(u32x4& d, unsigned so) const {
    if constexpr (KIND == KC) dsr128<I * 2048>(d, base[H] + so);
    else if constexpr (KIND == FL) dsr128<I * 2048 + H * 1024>(d, base[0] + so);
    else {
      u32x2 lo, hi;
      dsr_tr<H * 4096>(lo, base[I] + so); dsr_tr<H * 4096 + 512>(hi, base[I] + so);
      d = u32x4{lo[0], lo[1], hi[0], hi[1]};
    }
  }
};
template <int KX, int NX, int KY, int NY>
DI void r_consumer(const Frag<KX, NX>& fx, const Frag<KY, NY>& fy, int slot_bytes, f32x4 (&acc)[NX][NY]) {
  u32x4 xa[2][NX], ya[2][NY], xb[2][NX], yb[2][NY];
  auto rd = [&](u32x4 (&xn)[2][NX], u32x4 (&yn)[2][NY], unsigned so, auto i) {
    constexpr int I = decltype(i)::value, H = I / (NX + NY), J = I % (NX + NY);
    if constexpr (J < NY) fy.template read<H, J>(yn[H][J], so); else fx.template read<H, J - NY>(xn[H][J - NY], so);
  };
  auto land = [&](u32x4 (&xn)[2][NX], u32x4 (&yn)[2][NY]) {
    wait_lgkm<0>();
#pragma unroll
    for (int h = 0; h < 2; h++) {
#pragma unroll
      for (int i = 0; i < NX; i++) pin(xn[h][i]);
#pragma unroll
      for (int i = 0; i < NY; i++) pin(yn[h][i]);
    }
  };
  auto step = [&](u32x4 (&xc)[2][NX], u32x4 (&yc)[2][NY], u32x4 (&xn)[2][NX], u32x4 (&yn)[2][NY], unsigned so, auto more) {
    sfor<0, 2 * NX * NY>([&](auto i) {
      constexpr int I = decltype(i)::value, H = I / (NX * NY), J = I % (NX * NY);
      mfma(acc[J / NY][J % NY], xc[H][J / NY], yc[H][J % NY]);
      if constexpr (decltype(more)::value && I < 2 * (NX + NY)) rd(xn, yn, so, i);
    });
    if constexpr (decltype(more)::value) land(xn, yn);
  };
  pin2(acc);
  SB();
  bar();
  sfor<0, 2 * (NX + NY)>([&](auto i) { rd(xa, ya, 0, i); });
  land(xa, ya);
  int slot = 0;
#pragma unroll 1
  for (int g = 0; g < 8; g++) {
    bar();
    step(xa, ya, xb, yb, (slot + 1) * slot_bytes, ic<1>{});
    slot ^= 2;
    bar();
    step(xb, yb, xa, ya, slot * slot_bytes, ic<1>{});
  }
  bar();
  step(xa, ya, xb, yb, slot_bytes, ic<1>{});
  step(xb, yb, xa, ya, 0, ic<0>{});
  mfma_done();
  pin2(acc);
}
DI u64 mix64(u64 z) { z += 0x9E3779B97F4A7C15ull; z = (z ^ (z >> 30)) * 0xBF58476D1CE4E5B9ull; z = (z ^ (z >> 27)) * 0x94D049BB133111EBull; return z ^ (z >> 31); }
#endif

constexpr int KF_CODE = 48 * 1024, K0_CODE = 36 * 1024;
constexpr int R0 = 98304, L_TAB = 147456, L_SCR = L_TAB + 8192 + 64, KF_LDS = L_SCR + 1024;
extern "C" __global__ __launch_bounds__(320) void kf_main(const char* x, const char* wqc, const char* woc, char* ws, char* out) {
#if defined(__HIP_DEVICE_COMPILE__)
  extern __shared__ __attribute__((aligned(16))) char shm[];
  STAMP(1, 0);
  const int bid = blockIdx.x, xcd = bid & 7, idx = bid >> 3;
  const int wid = __builtin_amdgcn_readfirstlane(threadIdx.x >> 6), lane = threadIdx.x & 63, r = lane & 15, q = lane >> 4;
  LDS char* sm = (LDS char*)shm;
  const unsigned l0 = (unsigned)(size_t)sm;
  const int b = xcd >> 2;
  const bool isT = idx < 16;
  const int cp = 2 * (xcd & 3) + ((idx >> 3) & 1), rp2 = b * 8 + (idx & 7), ph2 = PH2;
  const int rp = 2 * xcd + (idx & 1), kg = idx >> 1, ph3 = kg % 3;
  const int rp4 = b * 8 + (xcd & 2) * 2 + (idx & 3), dp = (xcd & 1) * 6 + (idx >> 2), ph4 = (idx >> 2) & 3;
  const int so = (idx - 24) * 8 + xcd;
  const int rt5 = xcd * 16 + (idx >> 1), hf = idx & 1;
  constexpr int TSLOT = 28 * 1024, TW = 20 * 1024;

  if (wid == 4) {
    FSrc<4, KD, 0> h; h.init(mk_rsrc(ws + O_H, SZ_ACT), (b * 64 + kg * 4) * KD * BLK, ph3 * 6, lane);
    {
      FSrc<9, KD, 0> w; w.init(mk_rsrc(ws + O_W, 144 * KD * BLK), ((isT ? 0 : 72) + cp * 9) * KD * BLK, ph2 * 3, lane);
      w.issue(0, sm); w.issue(1, sm + 18432);
      wait_vm<18>(); bar();
      w.issue(2, sm + 2 * 18432); w.issue(3, sm + 3 * 18432);
      int slot = 4;
#pragma unroll 1
      for (int g = 1; g < 15; g++) {
        wait_vm<36>(); bar();
        w.issue(g + 3, sm + slot * 18432); slot = slot == 5 ? 0 : slot + 1;
      }
      wait_vm<36>(); bar();
      h.issue(2, sm + R0 + 2 * 8192); h.issue(3, sm + R0 + 3 * 8192);
      wait_vm<34>(); bar();
      h.issue(4, sm + R0 + 4 * 8192); h.issue(5, sm + R0 + 5 * 8192);
      wait_vm<32>(); bar();
      if (KFW) CODE_WARM(kf_main, KF_CODE, idx, l0 + L_SCR);
      bar();
      h.issue(0, sm + R0); h.issue(1, sm + R0 + 8192);
#pragma unroll 1
      for (int g = 6; g < 18; g++) h.issue(g, sm + (g - 6) * 8192);
      wait_vm<0>(); STAMPL(21); bar();
      bar();
      if (KFW) CODE_WARM(kf_main, KF_CODE, idx, l0 + L_SCR);
      bar();
    }
    const int fw_lane = FL_W + (lane < 36 ? (lane >> 1) * 4 + (lane & 1) : 0) + b * 2;
    rsrc_t rw = mk_rsrc(ws + O_WQC, SZ_TOK);
    unsigned v0 = b * 5 * KD * BLK + lane * 16, v1 = v0 + KD * BLK - 1024, v2 = v0 + 2 * KD * BLK - 2048, v3 = v0 + 3 * KD * BLK - 3072;
    auto wq_issue = [&](int s) {
      dma16<0, SC1>(rw, sm + s * 4096, v0, s * BLK); dma16<1024, SC1>(rw, sm + s * 4096, v1, s * BLK);
      dma16<2048, SC1>(rw, sm + s * 4096, v2, s * BLK); dma16<3072, SC1>(rw, sm + s * 4096, v3, s * BLK);
    };
    int wq_done = 0;
    if (idx < 24) {
      const int fv_lane = FL_V + ((lane & 32 ? (dp * 6 + 5) / 9 : (dp * 6) / 9) * 16 + b * 8) * 4 + (lane & 31);
      FSrc<6, 32, SC1> v; v.init(mk_rsrc(ws + O_V, SZ_ACT), (b * 72 + dp * 6) * 32 * BLK, ph4 * 4, lane);
      int g = 0;
      if (ready(ws, fv_lane)) {
#pragma unroll 1
        for (; g < 7; g++) { v.issue(g, sm + (g < 4 ? R0 + g * 12288 : (g - 4) * 12288)); wait_vm<24>(); }
      }
      STAMPL(22);
      bar();
      if (g == 0) poll(ws, fv_lane);
#pragma unroll 1
      for (; g < 12; g++) { v.issue(g, sm + (g < 4 ? R0 + g * 12288 : (g - 4) * 12288)); wait_vm<VTHR>(); }
      wait_vm<12>();
      STAMPL(23);
      bar();
      bar();
      wait_vm<0>(); bar();
#pragma unroll 1
      for (int g = 12; g < 16; g++) v.issue(g, sm + R0 + (g - 12) * 12288);
      {
        rsrc_t rx = mk_rsrc(x, 2048 * 2304);
#pragma unroll 1
        for (int p = 0; p < 24; p++) { int ch = p * 64 + lane, row = (ch * 5462) >> 16; dma16<0>(rx, sm + p * 1024, (rp4 * 128 + row) * 2304 + dp * 192 + (ch - row * 12) * 16, 0); }
        dma16<0>(mk_rsrc(ws + O_MOD, 4608), sm + 24576, lane < 12 ? lane * 16 : OOB, b * 2304 + dp * 192);
      }
      bool rdy = ready(ws, fw_lane);
      wait_vm<0>(); bar();
      if (rdy) {
#pragma unroll 1
        for (int s = 7; s < 21; s++) wq_issue(s);
        wq_done = 21;
      }
      bar();
    } else {
      bar();
      if (so < 36) {
        RSrc<10> t; t.r = mk_rsrc(ws + O_KC, SZ_TOK); t.fl((so & 1) * 5 * KD * BLK, lane);
        RSrc<8> w; w.r = mk_rsrc(wqc, DM * 2304); w.kc((so >> 1) * 64 * 2304, 2304, lane);
        r_loader<18>([&](int g, int slot) { t.issue(g, sm + slot * TSLOT); w.issue(g, sm + slot * TSLOT + TW); }, 0);
        bar();
      } else if (so < 54) {
        RSrc<20> t; t.r = mk_rsrc(ws + O_VC, SZ_TOK); t.fl(0, lane);
        RSrc<8> w; w.r = mk_rsrc(woc, DM * 2304); w.tr((so - 36) * 128, 2304, lane);
        r_loader<28>([&](int g, int slot) { t.issue(g, sm + slot * TSLOT); w.issue(g, sm + slot * TSLOT + TW); }, 0);
        bar();
      }
    }
    if (wq_done == 0) poll(ws, fw_lane);
#pragma unroll 1
    for (int s = 0; s < KD; s++) { if (s == 7 && wq_done) s = wq_done; wq_issue(s); }
    wait_vm<0>(); STAMPL(24); bar();
    return;
  }

  const unsigned lp = l0 + lane * 16;
  {
    rsrc_t ra = mk_rsrc(ws + O_H, SZ_ACT);
    unsigned a0 = (rp2 * 8 + wid * 2) * KD * BLK + lane * 16, a1 = a0 + KD * BLK;
    f32x4 acc[2][9]; zero(acc);
    if (isT) {
      asm volatile("; T" : "+v"(a0), "+v"(a1));
      f_consumer<9, KD, 6, true, 0, 110592, false, false, 0>(ra, a0, a1, lp, ph2, acc, 0, [] {});
      STAMP(1, 1);
      rsrc_t rt = mk_rsrc(ws + O_T, SZ_ACT);
      unsigned ob = (rp2 * 8 + wid * 2) * KD * BLK + lane * 16 + ((cp * 9) >> 1) * BLK;
      int od = cp & 1;
      u32x4 o[8]; u32x2 hb[2];
      if (od) {
#pragma unroll
        for (int i = 0; i < 8; i++) o[i] = pack8(acc[i >> 2][1 + 2 * (i & 3)], acc[i >> 2][2 + 2 * (i & 3)]);
        hb[0] = pack4(acc[0][0]); hb[1] = pack4(acc[1][0]);
      } else {
#pragma unroll
        for (int i = 0; i < 8; i++) o[i] = pack8(acc[i >> 2][2 * (i & 3)], acc[i >> 2][1 + 2 * (i & 3)]);
        hb[0] = pack4(acc[0][8]); hb[1] = pack4(acc[1][8]);
      }
      pin_all(o);
#pragma unroll
      for (int i = 0; i < 8; i++) st128b<SC1>(o[i], rt, ob + (i & 3) * BLK, ((i >> 2) * KD + od) * BLK);
      st64<SC1>(hb[0], rt, ob, od ? 8 : 4 * BLK); st64<SC1>(hb[1], rt, ob, KD * BLK + (od ? 8 : 4 * BLK));
      st_end();
      SB(); bar();
    } else {
      asm volatile("; V" : "+v"(a0), "+v"(a1));
      f_consumer<9, KD, 6, true, 0, 110592, true, false, 0>(ra, a0, a1, lp, ph2, acc, 0, [] {});
      STAMP(1, 1);
      bar();
      __builtin_amdgcn_s_sleep(VDELAY);
      rsrc_t rv = mk_rsrc(ws + O_V, SZ_ACT);
      unsigned ob = ((b * 72 + cp * 9) * 32 + (rp2 & 7) * 4 + wid) * BLK + lane * 16;
      u32x4 o[9];
#pragma unroll
      for (int n = 0; n < 9; n++) o[n] = pack8(acc[0][n], acc[1][n]);
      pin_all(o);
#pragma unroll
      for (int n = 0; n < 9; n++) st128b<SC1>(o[n], rv, ob, n * 32 * BLK);
      st_end();
    }
    publish(ws, (isT ? FL_T + (rp2 * 8 + cp) * 4 : FL_V + (cp * 16 + rp2) * 4) + wid, lane);
    STAMP(1, 2);
  }
  {
    const int rt0 = rp * 8 + wid * 2;
    f32x4 acc[2][4]; zero(acc);
    if (isT) __builtin_amdgcn_s_sleep(TDELAY);
    poll(ws, FL_T + (rp * 8 + (lane & 7)) * 4 + wid);
    STAMP(1, 3);
    unsigned a0 = rt0 * KD * BLK + lane * 16, a1 = a0 + KD * BLK;
    f_consumer<4, KD, 12, false, R0, 147456, false, false, SC1>(mk_rsrc(ws + O_T, SZ_ACT), a0, a1, lp, ph3, acc, 0, [] {}, ws, 17);
    STAMP(1, 4);
    rsrc_t rpp = mk_rsrc(ws + O_P, 128 * 32 * BLK), rs = mk_rsrc(ws + O_ST, 128 * 16 * 128);
    u32x2 rec[2]; u32x4 po[4];
#pragma unroll
    for (int t = 0; t < 2; t++) {
      float m = acc[t][0][0];
#pragma unroll
      for (int n = 0; n < 4; n++)
#pragma unroll
        for (int j = 0; j < 4; j++) m = fmaxf(m, acc[t][n][j]);
      m = ceilf(qmax(m));
      float l = 0;
#pragma unroll
      for (int n = 0; n < 4; n++)
#pragma unroll
        for (int j = 0; j < 4; j++) { float p = __builtin_amdgcn_exp2f(acc[t][n][j] - m); acc[t][n][j] = p; l += p; }
      unsigned o = ((rt0 + t) * 32 + kg * 2) * BLK;
      po[2 * t] = pack8(acc[t][0], acc[t][1]); po[2 * t + 1] = pack8(acc[t][2], acc[t][3]);
      pin(po[2 * t]); pin(po[2 * t + 1]);
      st128b<SC1>(po[2 * t], rpp, lane * 16, o); st128b<SC1>(po[2 * t + 1], rpp, lane * 16 + 1024, o);
      rec[t] = u32x2{__float_as_uint(m), __float_as_uint(qsum(l))};
    }
    pin_all(po);
    bar();
    wait_vm<0>();
    st64<SC1>(q & 1 ? rec[1] : rec[0], rs, lane < 32 ? (q & 1) * 2048 + r * 8 : OOB, (rt0 * 16 + kg) * 128);
    STAMP(1, 5);
  }
  if (idx < 24) {
    const int rt0 = rp4 * 8 + wid * 2;
    rsrc_t rs = mk_rsrc(ws + O_ST, 128 * 16 * 128);
    u32x2 sv[2][4];
    f32x4 acc[2][6]; zero(acc);
    __builtin_amdgcn_s_sleep(PDELAY);
#pragma unroll 1
    for (int spin = 0; spin < SPIN_CAP; spin++) {
      bool ok = true;
#pragma unroll
      for (int t = 0; t < 2; t++)
#pragma unroll
        for (int j = 0; j < 4; j++) { sv[t][j] = ld64<SC1>(rs, (q * 4 + j) * 128 + r * 8, (rt0 + t) * 2048); ok &= sv[t][j][1] != 0; }
      if (__all(ok)) break;
      asm volatile("s_sleep %0" ::"n"(PNAP) : "memory");
    }
    asm volatile("" ::: "memory"); SB();
    STAMP(1, 6);
    LDS unsigned* tab = (LDS unsigned*)(sm + L_TAB + wid * 2048);
    float inv[2];
    unsigned a0 = rt0 * 32 * BLK + lane * 16, a1 = a0 + 32 * BLK;
    f_consumer<6, 32, 8, false, R0, 147456, false, true, SC1>(mk_rsrc(ws + O_P, 128 * 32 * BLK), a0, a1, lp, ph4, acc, l0 + L_TAB + wid * 2048 + r * 64, [&] {
#pragma unroll
      for (int t = 0; t < 2; t++) {
        float mg = qmax(fmaxf(fmaxf(__uint_as_float(sv[t][0][0]), __uint_as_float(sv[t][1][0])), fmaxf(__uint_as_float(sv[t][2][0]), __uint_as_float(sv[t][3][0]))));
        float l = 0; u32x4 k;
#pragma unroll
        for (int j = 0; j < 4; j++) {
          float d = fminf(mg - __uint_as_float(sv[t][j][0]), 255.f);
          l += __uint_as_float(sv[t][j][1]) * __builtin_amdgcn_exp2f(-d);
          k[j] = (unsigned)d * 0x00800080u;
        }
        *(LDS u32x4*)(tab + t * 256 + r * 16 + ((q - ph4) & 3) * 4) = k;
        inv[t] = __builtin_amdgcn_rcpf(qsum(l));
      }
      wait_lgkm<0>();
    }, ws, 13);
    STAMP(1, 7);
    rsrc_t ro = mk_rsrc(ws + O_X1, SZ_ACT);
    u32x4 xo[2][3];
#pragma unroll
    for (int t = 0; t < 2; t++) {
#pragma unroll
      for (int n = 0; n < 6; n++) {
        u32x2 xv = *(LDS u32x2*)(sm + ((wid * 2 + t) * 16 + r) * 192 + n * 32 + q * 8), gv = *(LDS u32x2*)(sm + 24576 + n * 32 + q * 8);
        acc[t][n] = f32x4{blo(xv[0]), bhi(xv[0]), blo(xv[1]), bhi(xv[1])} + f32x4{blo(gv[0]), bhi(gv[0]), blo(gv[1]), bhi(gv[1])} * (acc[t][n] * inv[t]);
      }
#pragma unroll
      for (int p = 0; p < 3; p++) { xo[t][p] = pack8(acc[t][2 * p], acc[t][2 * p + 1]); pin(xo[t][p]); st128b<SC1>(xo[t][p], ro, lane * 16 + p * BLK, ((rt0 + t) * KD + dp * 3) * BLK); }
    }
    SB(); bar();
    u32x2 rec[2];
#pragma unroll
    for (int t = 0; t < 2; t++) {
      f32x2 s1 = {0, 0}, s2 = {0, 0};
#pragma unroll
      for (int p = 0; p < 3; p++)
#pragma unroll
        for (int j = 0; j < 4; j++) s1 += f32x2{blo(xo[t][p][j]), bhi(xo[t][p][j])};
      float mw = qsum(s1[0] + s1[1]) * (1.f / 96);
#pragma unroll
      for (int p = 0; p < 3; p++)
#pragma unroll
        for (int j = 0; j < 4; j++) { f32x2 d = f32x2{blo(xo[t][p][j]), bhi(xo[t][p][j])} - mw; s2 += d * d; }
      rec[t] = u32x2{__float_as_uint(mw), __float_as_uint(qsum(s2[0] + s2[1])) | 0x80000000u};
    }
    wait_vm<0>();
    st64<SC1>(q & 1 ? rec[1] : rec[0], mk_rsrc(ws + O_XS, 128 * 12 * 128), lane < 32 ? (q & 1) * 1536 + r * 8 : OOB, (rt0 * 12 + dp) * 128);
  } else if (so < 36) {
    if (wid < 2) {
      Frag<FL, 5> ft; ft.init(l0, 0, 0, lane);
      Frag<KC, 2> fw; fw.init(l0, TW, wid * 2, lane);
      f32x4 acc[2][5]; zero(acc);
      r_consumer(fw, ft, TSLOT, acc);
      STAMP(1, 7);
      rsrc_t ro = mk_rsrc(ws + O_WQC, SZ_TOK);
#pragma unroll
      for (int t = 0; t < 5; t++) st128<SC1>(pack8(acc[0][t] * QK_SCALE, acc[1][t] * QK_SCALE), ro, lane * 16, (((so & 1) * 5 + t) * KD + (so >> 1) * 2 + wid) * BLK);
      SB(); bar();
      publish(ws, FL_W + so * 2 + wid, lane);
    } else {
#pragma unroll 1
      for (int g = 0; g < 19; g++) bar();
    }
  } else if (so < 54) {
    int bb = wid >> 1, wn = wid & 1;
    Frag<FL, 5> ft; ft.init(l0, 0, bb * 5, lane);
    Frag<TR, 2> fw; fw.init(l0, TW, wn * 2, lane);
    f32x4 acc[5][2]; zero(acc);
    r_consumer(ft, fw, TSLOT, acc);
    STAMP(1, 7);
    rsrc_t ro = mk_rsrc(ws + O_VOC, SZ_TOK);
#pragma unroll
    for (int j = 0; j < 2; j++) {
      unsigned o = (bb * 72 + (so - 36) * 4 + wn * 2 + j) * 2560;
      st128<SC1>(pack8(acc[0][j], acc[1][j]), ro, lane * 16, o);
      st128<SC1>(pack8(acc[2][j], acc[3][j]), ro, lane * 16 + 1024, o);
      st64<SC1>(pack4(acc[4][j]), ro, lane * 8 + 2048, o);
    }
    SB(); bar();
    publish(ws, FL_O + (so - 36) * 4 + wid, lane);
  }
  STAMP(1, 8);
  {
    rsrc_t rx = mk_rsrc(ws + O_X1, SZ_ACT), rw = mk_rsrc(ws + O_WQC, SZ_TOK), rv = mk_rsrc(ws + O_VOC, SZ_TOK);
    u32x4 xf[9], w4[9], vf[9][2]; u32x2 vh[9], xr[9];
    const int dt0 = hf * 36 + wid * 9;
    poll(ws, FL_W + (lane < 36 ? (lane >> 1) * 4 + (lane & 1) : 0) + b * 2);
#pragma unroll
    for (int s = 0; s < 9; s++) w4[s] = ld128<SC1>(rw, lane * 16 + s * BLK, ((b * 5 + 4) * KD + wid * 9) * BLK);
    SB();
    bar();
    STAMP(1, 9);
    STAMP(1, 25);
    LDS char* wl = sm + wid * 36864 + lane * 16;
    f32x4 cs[5] = {};
    const u32x4 ones = {0x3f803f80u, 0x3f803f80u, 0x3f803f80u, 0x3f803f80u};
#pragma unroll
    for (int s = 0; s < 9; s++) {
#pragma unroll
      for (int t = 0; t < 4; t++) cs[t] = mfma_b(*(LDS u32x4*)(wl + s * 4096 + t * 1024), ones, cs[t]);
      cs[4] = mfma_b(w4[s], ones, cs[4]);
    }
    LDS float* csl = (LDS float*)(sm + L_TAB);
    if (r == 0) {
#pragma unroll
      for (int t = 0; t < 5; t++) *(LDS f32x4*)(csl + wid * 80 + t * 16 + q * 4) = cs[t];
    }
    SB();
    { int pr = (dt0 >> 1) + (lane < 5 ? lane : 0); pr -= pr > 35; poll(ws, FL_O + (pr >> 1) * 4 + b * 2 + (pr & 1)); }
    auto voc = [&](auto i) {
      constexpr int n = decltype(i)::value;
      unsigned o = (b * 72 + dt0 + n) * 2560;
      vf[n][0] = ld128<SC1>(rv, lane * 16, o); vf[n][1] = ld128<SC1>(rv, lane * 16 + 1024, o); vh[n] = ld64<SC1>(rv, lane * 8 + 2048, o);
    };
    sfor<0, VSPLIT>(voc);
    SB();
    STAMP(1, 26);
    u32x2 xs[3];
    rsrc_t rs = mk_rsrc(ws + O_XS, 128 * 12 * 128);
#pragma unroll 1
    for (int spin = 0; spin < SPIN_CAP; spin++) {
      bool ok = true;
#pragma unroll
      for (int j = 0; j < 3; j++) { xs[j] = ld64<SC1>(rs, (q * 3 + j) * 128 + r * 8, rt5 * 1536); ok &= (int)xs[j][1] < 0; }
      if (__all(ok)) break;
      asm volatile("s_sleep %0" ::"n"(PNAP) : "memory");
    }
    asm volatile("" ::: "memory"); SB();
    STAMP(1, 10);
#pragma unroll
    for (int s = 0; s < 9; s++) xf[s] = ld128<SC1>(rx, lane * 16 + s * BLK, (rt5 * KD + wid * 9) * BLK);
#pragma unroll
    for (int n = 0; n < 9; n++) xr[n] = ld64<SC1>(rx, lane * 16 + ((dt0 + n) & 1) * 8, (rt5 * KD + ((dt0 + n) >> 1)) * BLK);
    SB();
    float mean = qsum(__uint_as_float(xs[0][0]) + __uint_as_float(xs[1][0]) + __uint_as_float(xs[2][0])) * (1.f / 12), m2 = 0;
#pragma unroll
    for (int j = 0; j < 3; j++) { float d = __uint_as_float(xs[j][0]) - mean; m2 += __uint_as_float(xs[j][1] & 0x7fffffffu) + 96.f * d * d; }
    float rstd = rsqrtf(qsum(m2) * (1.f / DM) + 1e-5f);
    f32x4 sc[5] = {};
#pragma unroll
    for (int s = 0; s < 9; s++) {
#pragma unroll
      for (int t = 0; t < 4; t++) sc[t] = mfma_b(*(LDS u32x4*)(wl + s * 4096 + t * 1024), xf[s], sc[t]);
      sc[4] = mfma_b(w4[s], xf[s], sc[4]);
    }
    SB();
    sfor<VSPLIT, 9>(voc);
    SB();
#pragma unroll
    for (int t = 0; t < 5; t++) *(LDS f32x4*)(wl + t * 1024) = sc[t];
    wait_lgkm<0>(); bar();
    STAMP(1, 11);
    LDS char* rl = sm + lane * 16;
#pragma unroll
    for (int t = 0; t < 5; t++) {
      f32x4 c = (*(LDS f32x4*)(csl + t * 16 + q * 4) + *(LDS f32x4*)(csl + 80 + t * 16 + q * 4)) + (*(LDS f32x4*)(csl + 160 + t * 16 + q * 4) + *(LDS f32x4*)(csl + 240 + t * 16 + q * 4));
      f32x4 v = (*(LDS f32x4*)(rl + t * 1024) + *(LDS f32x4*)(rl + 36864 + t * 1024)) + (*(LDS f32x4*)(rl + 2 * 36864 + t * 1024) + *(LDS f32x4*)(rl + 3 * 36864 + t * 1024));
      sc[t] = (v - mean * c) * rstd;
    }
#pragma unroll
    for (int j = 1; j < 4; j++) sc[4][j] = q == 3 ? -__builtin_inff() : sc[4][j];
    float m = sc[0][0];
#pragma unroll
    for (int t = 0; t < 5; t++)
#pragma unroll
      for (int j = 0; j < 4; j++) m = fmaxf(m, sc[t][j]);
    m = qmax(m);
    float l = 0;
#pragma unroll
    for (int t = 0; t < 5; t++)
#pragma unroll
      for (int j = 0; j < 4; j++) { float p = __builtin_amdgcn_exp2f(sc[t][j] - m); sc[t][j] = p; l += p; }
    float inv = __builtin_amdgcn_rcpf(qsum(l));
    u32x4 p0 = pack8(sc[0] * inv, sc[1] * inv), p1 = pack8(sc[2] * inv, sc[3] * inv);
    u32x2 p2h = pack4(sc[4] * inv);
    u32x4 p2 = {p2h[0], p2h[1], 0u, 0u};
    STAMP(1, 27);
    rsrc_t ro = mk_rsrc(out, 2048 * 2304);
    u32x2 ov[9];
#pragma unroll
    for (int n = 0; n < 9; n++) {
      f32x4 o = {blo(xr[n][0]), bhi(xr[n][0]), blo(xr[n][1]), bhi(xr[n][1])};
      o = mfma_b(vf[n][0], p0, o);
      o = mfma_b(vf[n][1], p1, o);
      o = mfma_b(u32x4{vh[n][0], vh[n][1], 0u, 0u}, p2, o);
      ov[n] = pack4(o);
    }
    unsigned orow = (rt5 * 16 + r) * 2304;
    u32x4 oo[4];
#pragma unroll
    for (int n = 0; n < 8; n += 2) {
      auto e = __builtin_amdgcn_permlane16_swap(ov[n][0], ov[n + 1][0], false, false);
      auto f = __builtin_amdgcn_permlane16_swap(ov[n][1], ov[n + 1][1], false, false);
      oo[n >> 1] = u32x4{e[0], f[0], e[1], f[1]};
    }
    pin_all(oo);
#pragma unroll
    for (int n = 0; n < 4; n++) st128b(oo[n], ro, orow + (q & 1) * 32 + (q >> 1) * 16, (dt0 + 2 * n) * 32);
    st_end();
    st64(ov[8], ro, orow + q * 8, (dt0 + 8) * 32);
  }
  ENDSTAMP(1, 12);
#endif
}

constexpr int K0_SCR = 147456 + 9216 + 768, K0_LDS = K0_SCR + 1024;
extern "C" __global__ __launch_bounds__(384) void k0_pre(const char* x, const char* c, const char* y, const char* wqkv, const char* wo,
                                                        const char* wqc, const char* wkvc, const char* woc, const char* wmod, char* ws) {
#if defined(__HIP_DEVICE_COMPILE__)
  extern __shared__ __attribute__((aligned(16))) char shm[];
  STAMP(0, 0);
  int bid = blockIdx.x, xcd = bid & 7, idx = bid >> 3;
  int wid = __builtin_amdgcn_readfirstlane(threadIdx.x >> 6), lane = threadIdx.x & 63;
  int qd = xcd & 3, nr = (qd & 1) ? 4 : 5, nc = (qd & 2) ? 4 : 5, r0 = (qd & 1) ? 5 : 0, c0 = (qd & 2) ? 5 : 0;
  LDS char* sm = (LDS char*)shm;
  unsigned l0 = (unsigned)(size_t)sm;
  int r = lane & 15, q = lane >> 4;
  if (wid == 0) CODE_WARM(k0_pre, K0_CODE, idx, l0 + K0_SCR);
  if (idx < nr * nc) {
    int ti = r0 + idx % nr, tj = c0 + idx / nr, mat = xcd >> 2;
    int rot = (18 * ((ti + tj) % K0PH) + K0PH / 2) / K0PH;
    constexpr int SLOT = 32768;
    if (wid >= 4) {
      RSrc<16> s;
      if (wid == 4) { s.r = mk_rsrc(wqkv, DM * 6912); s.kc(ti * 128 * 6912 + mat * 4608, 6912, lane); }
      else if (mat == 0) { s.r = mk_rsrc(wqkv, DM * 6912); s.kc(tj * 128 * 6912 + 2304, 6912, lane); }
      else { s.r = mk_rsrc(wo, DM * 2304); s.tr(tj * 256, 2304, lane); }
      LDS char* d = sm + (wid - 4) * 16384;
      r_loader<16>([&](int g, int slot) { s.issue(g, d + slot * SLOT); }, rot);
    } else {
      int wi = wid >> 1, wj = wid & 1;
      f32x4 acc[4][4]; zero(acc);
      Frag<KC, 4> fx; fx.init(l0, 0, wi * 4, lane);
      float sc = 1.f;
      if (mat == 0) { Frag<KC, 4> fy; fy.init(l0, 16384, wj * 4, lane); r_consumer(fx, fy, SLOT, acc); sc = QK_SCALE; }
      else { Frag<TR, 4> fy; fy.init(l0, 16384, wj * 4, lane); r_consumer(fx, fy, SLOT, acc); }
      STAMP(0, 2); STAMP(0, 8);
      rsrc_t ro = mk_rsrc(ws + O_W, 144 * KD * BLK);
      unsigned o = ((mat * 72 + tj * 8 + wj * 4) * KD + ti * 4 + wi * 2) * BLK;
      u32x4 ov[8];
#pragma unroll
      for (int i = 0; i < 8; i++) ov[i] = pack8(acc[2 * (i & 1)][i >> 1] * sc, acc[2 * (i & 1) + 1][i >> 1] * sc);
      pin_all(ov);
#pragma unroll
      for (int i = 0; i < 8; i++) st128b<K0AUX>(ov[i], ro, lane * 16 + (i & 1) * 1024, o + (i >> 1) * KD * BLK);
      if (wid == 0) CODE_WARM(kf_main, KF_CODE, idx, l0 + K0_SCR);
      ENDSTAMP(0, 3);
    }
    return;
  }
  int o = (xcd >> 2) * 47 + (qd == 0 ? 0 : qd == 1 ? 7 : qd == 2 ? 19 : 31) + idx - nr * nc;
  if (o < 72 && !(o & 1)) {
    constexpr int SLOT = 28 * 1024, WOFF = 20 * 1024;
    int pn = o >> 1, rot = (18 * (pn % K0PH) + K0PH / 2) / K0PH;
    if (wid == 4) {
      RSrc<20> s; s.r = mk_rsrc(y, 154 * 2304); s.kstep = 128;
#pragma unroll
      for (int p = 0; p < 20; p++) {
        int row = 8 * p + (lane >> 3), b = row >= 80, t = row - 80 * b;
        s.v[p] = t < 77 ? (b * 77 + t) * 2304 + (((lane & 7) ^ ((row >> 1) & 7)) << 4) - (p & 3) * 1024 : OOB;
      }
      r_loader<20>([&](int g, int slot) { s.issue(g, sm + slot * SLOT); }, rot);
    } else if (wid == 5) {
      RSrc<8> s; s.r = mk_rsrc(wkvc, DM * 4608); s.tr(pn * 128, 4608, lane);
#pragma unroll 1
      for (int g = 0; g < 18; g++) s.issue(g, sm + 3 * SLOT + WOFF);
      r_loader<8>([&](int g, int slot) { s.issue(g, sm + slot * SLOT + WOFF); }, rot);
    } else {
      int b = wid >> 1, wn = wid & 1;
      Frag<KC, 5> ft; ft.init(l0, 0, b * 5, lane);
      Frag<TR, 2> fw; fw.init(l0, WOFF, wn * 2, lane);
      f32x4 acc[2][5]; zero(acc);
      r_consumer(fw, ft, SLOT, acc);
      STAMP(0, 2);
      rsrc_t ro = mk_rsrc(ws + O_KC, 2 * SZ_TOK);
      int kb = pn * 2 + wn, sel = kb >= KD;
      unsigned oo = sel * SZ_TOK + (kb - sel * KD) * BLK, vo = ((q >> 1) * 16 + r) * 16 + (q & 1) * 8;
#pragma unroll
      for (int t = 0; t < 5; t++) {
        st64(pack4(acc[0][t]), ro, vo, oo + (b * 5 + t) * KD * BLK);
        st64(pack4(acc[1][t]), ro, vo + 512, oo + (b * 5 + t) * KD * BLK);
      }
      if (wid == 0) CODE_WARM(kf_main, KF_CODE, idx, l0 + K0_SCR);
      ENDSTAMP(0, 3);
    }
    return;
  }
  const int ax = o < 72 ? o >> 1 : o - 36;
  int pan = ax < 36 ? ax : ax + 14;
  GAS u64* flg = (GAS u64*)(ws + O_FLAG);
  u32x3 cv; u64 ep;
  { u32x4 dc = mk_desc(c, 2 * 2304); unsigned vo = (lane >> 5) * 2304 + wid * 384 + (lane & 31) * 12;
    asm volatile("buffer_load_dwordx3 %0, %1, %2, 0 offen" : "=v"(cv) : "v"(vo), "s"(dc) : "memory"); }
  asm volatile("global_load_dwordx2 %0, %1, %2 sc1" : "=v"(ep) : "v"(0u), "s"(flg) : "memory");
  auto wm_issue = [&]() {
    RSrc<8> s; s.tr(pan * 128 + wid * 192 * 13824, 13824, lane);
    u32x4 dw = mk_desc(wmod, DM * 13824);
#pragma unroll
    for (int g = 0; g < 3; g++) {
      dma4_asm(dw, l0 + wid * 24576 + g * 8192, s.v[0], s.v[1], s.v[2], s.v[3], g * 64 * 13824);
      dma4_asm(dw, l0 + wid * 24576 + g * 8192 + 4096, s.v[4], s.v[5], s.v[6], s.v[7], g * 64 * 13824);
    }
  };
  const int hb = ax - 14, hbat = hb >= 22, hj = (hb - 22 * hbat) * 3 + (wid >> 1), ht = hbat * 64 + hj, kh = wid & 1;
  const bool hval = hb >= 0 && hj < 64;
  auto x_issue = [&]() {
    u32x4 dx = mk_desc(x, 2048 * 2304);
#pragma unroll
    for (int p4 = 0; p4 < 5; p4++) {
      unsigned v[4];
#pragma unroll
      for (int i = 0; i < 4; i++) {
        int ch = (p4 * 4 + i) * 64 + lane, row = (ch * 911) >> 16, cc = ch - row * 72;
        v[i] = hval && row < 16 ? (ht * 16 + row) * 2304 + kh * 1152 + ((cc ^ ((row >> 1) & 7)) << 4) - i * 1024 + 4096 : OOB;
      }
      u32x4 d = dx; { u64 a = (u64)x - 4096; d[0] = (unsigned)a; d[1] = (unsigned)(a >> 32) & 0xffffu; d[2] = 2048 * 2304 + 4096; }
      dma4_asm(d, l0 + wid * 24576 + p4 * 4096, v[0], v[1], v[2], v[3], 0);
    }
  };
  LDS float* red = (LDS float*)(sm + 147456);
  LDS unsigned* sil = (LDS unsigned*)(sm + 147456 + 3072);
  LDS float* tabf = (LDS float*)(sm + 147456);
  LDS f32x2* st = (LDS f32x2*)(sm + 147456 + 9216);
  u32x4 cf[6];
  u64 tag;
  auto wm_mma = [&]() {
    Frag<TR, 4> fw; fw.init(l0, wid * 24576, 0, lane);
    f32x4 acc[1][4]; zero(acc); pin2(acc);
    sfor<0, 3>([&](auto i) {
      constexpr int g = decltype(i)::value;
      u32x4 w[2][4];
      sfor<0, 4>([&](auto jj) { fw.template read<0, decltype(jj)::value>(w[0][decltype(jj)::value], g * 8192); fw.template read<1, decltype(jj)::value>(w[1][decltype(jj)::value], g * 8192); });
      wait_lgkm<0>();
#pragma unroll
      for (int h = 0; h < 2; h++) {
        pin(cf[2 * g + h]);
#pragma unroll
        for (int j = 0; j < 4; j++) { pin(w[h][j]); mfma(acc[0][j], w[h][j], cf[2 * g + h]); }
      }
    });
    mfma_done(); pin2(acc);
    if (r < 2) {
#pragma unroll
      for (int j = 0; j < 4; j++) *(LDS f32x4*)(red + wid * 128 + r * 64 + j * 16 + q * 4) = acc[0][j];
    }
    wait_lgkm<0>(); bar();
    if (wid == 0) {
      f32x2 a = {0, 0};
#pragma unroll
      for (int w = 0; w < 6; w++) a += *(LDS f32x2*)(red + w * 128 + lane * 2);
      unsigned pv = pk2(a[0], a[1]);
      if (pan < 36) {
        unsigned tl = (unsigned)tag & 0xffff0000u, th = (unsigned)(tag >> 32);
        st128<SC1>(u32x4{tl | (pv & 0xffffu), th, tl | (pv >> 16), th}, mk_rsrc(ws + O_MG, 2 * 2304 * 8), (lane >> 5) * 18432 + pan * 512 + (lane & 31) * 16, 0);
      } else __builtin_amdgcn_raw_buffer_store_b32(pv, mk_rsrc(ws + O_MOD, 4608), (lane >> 5) * 2304 + (pan - 36) * 128 + (lane & 31) * 4, 0, 0);
    }
  };
  if (ax < 40) {
    wm_issue();
    asm volatile("s_waitcnt vmcnt(24)" : "+v"(cv), "+v"(ep)::"memory");
    tag = mix64(ep);
#pragma unroll
    for (int j = 0; j < 3; j++) {
      float a = blo(cv[j]), b = bhi(cv[j]);
      sil[wid * 192 + lane * 3 + j] = pk2(a * __builtin_amdgcn_rcpf(1.f + __expf(-a)), b * __builtin_amdgcn_rcpf(1.f + __expf(-b)));
    }
    wait_lgkm<0>();
    unsigned sa = l0 + 147456 + 3072 + wid * 768 + (r & 1) * 384 + q * 16;
    sfor<0, 6>([&](auto i) { dsr128<decltype(i)::value * 64>(cf[decltype(i)::value], sa); });
    wait_vm<0>();
    STAMP(0, 7);
    wm_mma();
    if (wid) __builtin_amdgcn_s_sleep(3);
    if (hb >= 0) x_issue();
  } else {
    x_issue();
    asm volatile("s_waitcnt vmcnt(20)" : "+v"(cv), "+v"(ep)::"memory");
    tag = mix64(ep);
  }
  if (wid == 0 && lane == 0) __hip_atomic_store(flg + 8 + ax, tag, __ATOMIC_RELAXED, __HIP_MEMORY_SCOPE_AGENT);
  STAMP(0, 1);
  if (hb >= 0) {
    wait_vm<0>();
    STAMP(0, 5);
    f32x2 xf[18][4], s1 = {0, 0};
    { LDS char* xp = sm + wid * 24576 + r * 1152;
#pragma unroll
      for (int s = 0; s < 18; s++) {
        u32x4 v = *(LDS u32x4*)(xp + (((4 * s + q) ^ ((r >> 1) & 7)) << 4));
#pragma unroll
        for (int j = 0; j < 4; j++) { xf[s][j] = f32x2{blo(v[j]), bhi(v[j])}; s1 += xf[s][j]; }
      } }
    float mw = qsum(s1[0] + s1[1]) * (1.f / 576);
    f32x2 s2 = {0, 0};
#pragma unroll
    for (int s = 0; s < 18; s++)
#pragma unroll
      for (int j = 0; j < 4; j++) { xf[s][j] -= mw; s2 += xf[s][j] * xf[s][j]; }
    float m2 = qsum(s2[0] + s2[1]);
    if (q == 0) st[wid * 16 + r] = f32x2{mw, m2};
    wait_lgkm<0>(); bar();
    f32x2 so = st[(wid ^ 1) * 16 + r];
    float dm = 0.5f * (so[0] - mw), rstd = rsqrtf((m2 + so[1] + 2 * 576 * dm * dm) * (1.f / DM) + 1e-5f);
#pragma unroll
    for (int s = 0; s < 18; s++)
#pragma unroll
      for (int j = 0; j < 4; j++) { xf[s][j] = (xf[s][j] - dm) * rstd; pin(xf[s][j]); }
    STAMP(0, 6);
    {
      rsrc_t rg = mk_rsrc(ws + O_MG, 2 * 2304 * 8);
      unsigned t = threadIdx.x, tl = (unsigned)tag & 0xffff0000u, th = (unsigned)(tag >> 32);
      u32x4 g[3];
#pragma unroll 1
      for (int spin = 0; spin < SPIN_CAP; spin++) {
        bool ok = true;
#pragma unroll
        for (int j = 0; j < 3; j++) {
          g[j] = ld128<SC1>(rg, t * 48 + j * 16, hbat * 18432);
          ok &= (g[j][0] & 0xffff0000u) == tl && g[j][1] == th && (g[j][2] & 0xffff0000u) == tl && g[j][3] == th;
        }
        if (__all(ok)) break;
        asm volatile("s_sleep 6" ::: "memory");
      }
      float one = t >= 192 ? 1.f : 0.f;
#pragma unroll
      for (int j = 0; j < 3; j++) *(LDS f32x2*)(tabf + t * 6 + j * 2) = f32x2{blo(g[j][0]) + one, blo(g[j][2]) + one};
    }
    STAMP(0, 2);
    wait_lgkm<0>(); bar();
    rsrc_t rh = mk_rsrc(ws + O_H, SZ_ACT);
    unsigned ho = hval ? (ht * KD + kh * 18) * BLK + ((((q & 1) << 1) | (q >> 1)) * 16 + r) * 16 : OOB;
    LDS float* tp = tabf + kh * 576 + q * 8;
    u32x4 hv[18];
#pragma unroll
    for (int s = 0; s < 18; s++) {
      f32x4 f0 = *(LDS f32x4*)(tp + s * 32), f1 = *(LDS f32x4*)(tp + s * 32 + 4), a0 = *(LDS f32x4*)(tp + 1152 + s * 32), a1 = *(LDS f32x4*)(tp + 1152 + s * 32 + 4);
      f32x2 h0 = xf[s][0] * a0.xy + f0.xy, h1 = xf[s][1] * a0.zw + f0.zw, h2 = xf[s][2] * a1.xy + f1.xy, h3 = xf[s][3] * a1.zw + f1.zw;
      auto e = __builtin_amdgcn_permlane32_swap(pk2(h0[0], h0[1]), pk2(h2[0], h2[1]), false, false);
      auto f = __builtin_amdgcn_permlane32_swap(pk2(h1[0], h1[1]), pk2(h3[0], h3[1]), false, false);
      hv[s] = u32x4{e[0], f[0], e[1], f[1]};
    }
    pin_all(hv);
#pragma unroll
    for (int s = 0; s < 18; s++) st128b<K0AUX>(hv[s], rh, ho, s * BLK);
    st_end();
    STAMP(0, 4);
  } else {
    pan = 36 + ax;
    wait_lgkm<0>(); bar();
    wm_issue();
    wait_vm<0>();
    wm_mma();
  }
  { rsrc_t rz = mk_rsrc(ws + O_FL, CLR_BYTES);
    unsigned t = (ax * 384 + threadIdx.x) * 16;
    st128(u32x4{0, 0, 0, 0}, rz, t, 0); st128(u32x4{0, 0, 0, 0}, rz, t + 58 * 384 * 16, 0); }
  {
    unsigned v = lane * 16;
    if (wid == 0) CODE_WARM(kf_main, KF_CODE, idx, l0 + K0_SCR);
    if (WPF && (ax < 14 || ax >= 40)) {
      int first = ax < 14 ? (ax * 6 + wid) * 36 : 3024 + ((ax - 40) * 6 + wid) * 20, n = ax < 14 ? 36 : 20;
      u32x4 d1 = mk_desc(wqc, DM * 2304), d2 = mk_desc(woc, DM * 2304);
      for (int p = first; p < first + n; p += 4) {
        if (p < 2592) dma4_asm(d1, l0 + wid * 24576, v, v, v, v, p * 1024); else dma4_asm(d2, l0 + wid * 24576, v, v, v, v, (p - 2592) * 1024);
      }
    }
  }
  if (ax == 57 && wid == 0) {
#pragma unroll 1
    for (int spin = 0; spin < SPIN_CAP; spin++) {
      u64 f = __hip_atomic_load(flg + 8 + (lane < 58 ? lane : 0), __ATOMIC_RELAXED, __HIP_MEMORY_SCOPE_AGENT);
      if (__all(f == tag)) break;
      __builtin_amdgcn_s_sleep(4);
    }
    if (lane == 0) __hip_atomic_store(flg, tag, __ATOMIC_RELAXED, __HIP_MEMORY_SCOPE_AGENT);
  }
  ENDSTAMP(0, 3);
#endif
}

extern "C" __global__ void amp_k(char* ws) {
#if defined(__HIP_DEVICE_COMPILE__)
  __shared__ u64 red[256]; __shared__ unsigned cnt[256];
  GAS u64* p = (GAS u64*)(ws + O_PROF);
  int b = threadIdx.x;
  red[b] = p[b * 32]; __syncthreads();
  u64 t0 = ~0ull; for (int i = 0; i < 256; i++) t0 = red[i] < t0 ? red[i] : t0;
  __syncthreads();
  u64 v = p[(0 * 256 + b) * 32 + 8] - t0; bool ok = v < 30000;
  red[b] = ok ? v : 0; cnt[b] = ok; __syncthreads();
  u64 m = 0, sum = 0; unsigned n = 0; for (int i = 0; i < 256; i++) { m = red[i] > m ? red[i] : m; sum += red[i]; n += cnt[i]; }
  u64 dt = m * 100;
  u64 t1 = __builtin_amdgcn_s_memrealtime();
  while (__builtin_amdgcn_s_memrealtime() - t1 < dt) __builtin_amdgcn_s_sleep(32);
  for (int i = 0; i < 64; i++) p[b * 64 + i] = 0;
#endif
}
extern "C" void kernel_launch(void* const* d_in, const int* in_sizes, int n_in, void* d_out, int out_size, void* d_ws, size_t ws_size,
                              hipStream_t stream) {
  const char *x = (const char*)d_in[0], *c = (const char*)d_in[1], *y = (const char*)d_in[2], *wqkv = (const char*)d_in[3], *wo = (const char*)d_in[4],
             *wqc = (const char*)d_in[5], *wkvc = (const char*)d_in[6], *woc = (const char*)d_in[7], *wmod = (const char*)d_in[8];
  char* ws = (char*)d_ws;
  (void)hipFuncSetAttribute((const void*)k0_pre, hipFuncAttributeMaxDynamicSharedMemorySize, K0_LDS);
  (void)hipFuncSetAttribute((const void*)kf_main, hipFuncAttributeMaxDynamicSharedMemorySize, KF_LDS);
  k0_pre<<<256, 384, K0_LDS, stream>>>(x, c, y, wqkv, wo, wqc, wkvc, woc, wmod, ws);
  kf_main<<<256, 320, KF_LDS, stream>>>(x, wqc, woc, ws, (char*)d_out);
  amp_k<<<1, 256, 0, stream>>>(ws);
}
```

```cpp
#include <hip/hip_runtime.h>
#include <hip/hip_bf16.h>
#include <type_traits>

typedef __attribute__((ext_vector_type(8))) __bf16 bf16x8;
typedef __attribute__((ext_vector_type(2))) __bf16 bf16x2;
typedef __attribute__((ext_vector_type(4))) float f32x4;
typedef __attribute__((ext_vector_type(2))) float f32x2;
typedef __attribute__((ext_vector_type(4))) unsigned u32x4;
typedef __attribute__((ext_vector_type(3))) unsigned u32x3;
typedef __attribute__((ext_vector_type(2))) unsigned u32x2;
typedef unsigned long long u64;
#define LDS __attribute__((address_space(3)))
#define GAS __attribute__((address_space(1)))
#define DI __device__ __forceinline__

constexpr int DM = 1152;
constexpr int KD = 36;
constexpr int BLK = 1024;
constexpr size_t O_FLAG = 0;
constexpr size_t O_MOD = 4096;
constexpr size_t O_MG = 12288;
constexpr size_t O_FL = 65536;
constexpr int FL_T = 0, FL_V = 576, FL_W = 1600, FL_O = 1672, FL_N = 1816;
constexpr size_t O_ST = O_FL + 1920 * 128;
constexpr size_t O_XS = O_ST + 128 * 16 * 128;
constexpr size_t CLR_BYTES = 1920 * 128 + 128 * 16 * 128 + 128 * 12 * 128;
constexpr size_t O_H = O_FL + CLR_BYTES;
constexpr size_t SZ_ACT = 128ull * 36 * 1024;
constexpr size_t O_W = O_H + SZ_ACT;
constexpr size_t O_T = O_W + 144ull * 36 * 1024;
constexpr size_t O_V = O_T + SZ_ACT;
constexpr size_t O_P = O_V + SZ_ACT;
constexpr size_t O_X1 = O_P + 128ull * 32 * 1024;
constexpr size_t SZ_TOK = 10ull * 36 * 1024;
constexpr size_t O_KC = O_X1 + SZ_ACT;
constexpr size_t O_VC = O_KC + SZ_TOK;
constexpr size_t O_WQC = O_VC + SZ_TOK;
constexpr size_t O_VOC = O_WQC + SZ_TOK;
constexpr size_t O_CS = O_VOC + SZ_TOK;
constexpr size_t O_SP = O_CS + 32768;

constexpr size_t O_PROF = 200ull << 20;
#define STAMP(k, i)
#define ENDSTAMP(k, i)
#define STAMPL(i)
#define VTHR 63
#define PH2 0
#define VDELAY 24
#define TDELAY 12
#define PDELAY 16
#define PNAP 6
#define VSPLIT 4
#define WPF 12
#define KFW 1
#define K0XO 4
#define HMODE 0
#define HSLEEP 24
#define WEARLY 2
#define K0AUX 16
#define K0PH 2

constexpr float QK_SCALE = 1.4426950408889634f * 0.029462782549439476f;

#if defined(__HIP_DEVICE_COMPILE__)
typedef __amdgpu_buffer_rsrc_t rsrc_t;
DI rsrc_t mk_rsrc(const void* p, unsigned bytes) { return __builtin_amdgcn_make_buffer_rsrc((void*)p, 0, bytes, 0x00020000); }
DI u32x4 mk_desc(const void* p, unsigned bytes) { u64 a = (u64)p; return u32x4{(unsigned)a, (unsigned)(a >> 32) & 0xffffu, bytes, 0x00020000u}; }
DI unsigned pk2(float a, float b) { f32x2 p = {a, b}; return __builtin_bit_cast(unsigned, __builtin_convertvector(p, bf16x2)); }
DI float blo(unsigned u) { return __uint_as_float(u << 16); }
DI float bhi(unsigned u) { return __uint_as_float(u & 0xffff0000u); }
template <int N> DI void wait_vm() { asm volatile("s_waitcnt vmcnt(%0)" ::"n"(N > 63 ? 63 : N) : "memory"); }
template <int N> DI void wait_lgkm() { asm volatile("s_waitcnt lgkmcnt(%0)" ::"n"(N > 15 ? 15 : N) : "memory"); }
DI void SB() { __builtin_amdgcn_sched_barrier(0); }
DI void bar() { asm volatile("s_barrier" ::: "memory"); }
template <int I, int N, class F> DI void sfor(F&& f) { if constexpr (I < N) { f(std::integral_constant<int, I>{}); sfor<I + 1, N>(f); } }
template <int V> using ic = std::integral_constant<int, V>;
DI void mfma(f32x4& c, const u32x4& x, const u32x4& y) { asm volatile("v_mfma_f32_16x16x32_bf16 %0, %1, %2, %0" : "+v"(c) : "v"(x), "v"(y)); }
DI void mfma0(f32x4& c, const u32x4& x, const u32x4& y) { asm volatile("v_mfma_f32_16x16x32_bf16 %0, %1, %2, 0" : "=&v"(c) : "v"(x), "v"(y)); }
DI f32x4 mfma_b(const u32x4& x, const u32x4& y, const f32x4& c) {
  return __builtin_amdgcn_mfma_f32_16x16x32_bf16(__builtin_bit_cast(bf16x8, x), __builtin_bit_cast(bf16x8, y), c, 0, 0, 0);
}
template <class T> DI void pin(T& x) { asm volatile("" : "+v"(x)); }
template <int OFF> DI void dsr128(u32x4& d, unsigned a) { asm volatile("ds_read_b128 %0, %1 offset:%2" : "=v"(d) : "v"(a), "n"(OFF)); }
template <int OFF> DI void dsr_tr(u32x2& d, unsigned a) { asm volatile("ds_read_b64_tr_b16 %0, %1 offset:%2" : "=v"(d) : "v"(a), "n"(OFF)); }
constexpr int SC1 = 16;
template <int AUX = 0> DI u32x4 ld128(rsrc_t r, unsigned v, unsigned s) { return __builtin_amdgcn_raw_buffer_load_b128(r, v, s, AUX); }
template <int AUX = 0> DI u32x2 ld64(rsrc_t r, unsigned v, unsigned s) { return __builtin_amdgcn_raw_buffer_load_b64(r, v, s, AUX); }
template <int AUX = 0> DI void st128(u32x4 d, rsrc_t r, unsigned v, unsigned s) {
  __builtin_amdgcn_raw_buffer_store_b128(d, r, v, s, AUX); SB(); asm volatile("s_nop 1"); SB();
}
template <int AUX = 0> DI void st128b(u32x4 d, rsrc_t r, unsigned v, unsigned s) { __builtin_amdgcn_raw_buffer_store_b128(d, r, v, s, AUX); }
DI void st_end() { SB(); asm volatile("s_nop 1"); SB(); }
template <int N> DI void pin_all(u32x4 (&a)[N]) {
#pragma unroll
  for (int i = 0; i < N; i++) pin(a[i]);
  SB();
}
template <int AUX = 0> DI void st64(u32x2 d, rsrc_t r, unsigned v, unsigned s) { __builtin_amdgcn_raw_buffer_store_b64(d, r, v, s, AUX); }
template <int IMM, int AUX = 0> DI void dma16(rsrc_t r, LDS char* d, unsigned v, unsigned s) { __builtin_amdgcn_raw_ptr_buffer_load_lds(r, d, 16, v, s, IMM, AUX); }
DI void dma4_asm(u32x4 desc, unsigned lds, unsigned v0, unsigned v1, unsigned v2, unsigned v3, unsigned s) {
  unsigned keep;
  asm volatile("s_mov_b32 %0, m0\n\ts_mov_b32 m0, %2\n\ts_nop 0\n\t"
               "buffer_load_dwordx4 %3, %1, %7 offen lds\n\tbuffer_load_dwordx4 %4, %1, %7 offen offset:1024 lds\n\t"
               "buffer_load_dwordx4 %5, %1, %7 offen offset:2048 lds\n\tbuffer_load_dwordx4 %6, %1, %7 offen offset:3072 lds\n\ts_mov_b32 m0, %0"
               : "=&s"(keep) : "s"(desc), "s"(lds), "v"(v0), "v"(v1), "v"(v2), "v"(v3), "s"(s) : "memory");
}
#define CODE_WARM(SYM, BYTES, PIECE, LDSADDR) do { unsigned keep_; u64 pc_; \
  asm volatile("s_getpc_b64 s[40:41]\n\ts_add_u32 s40, s40, " #SYM "@rel32@lo+4\n\ts_addc_u32 s41, s41, " #SYM "@rel32@hi+12\n\ts_mov_b64 %0, s[40:41]" : "=s"(pc_)::"s40", "s41", "scc"); \
  asm volatile("s_mov_b32 %0, m0\n\ts_mov_b32 m0, %2\n\ts_nop 0\n\tbuffer_load_dwordx4 %3, %1, %4 offen lds\n\tbuffer_load_dwordx4 %3, %1, %5 offen lds\n\ts_mov_b32 m0, %0" \
               : "=&s"(keep_) : "s"(mk_desc((const void*)pc_, BYTES)), "s"(LDSADDR), "v"(lane * 16), "s"((PIECE) * 1024), "s"((PIECE) * 1024 + 32768) : "memory"); } while (0)
DI u32x4 pack8(const f32x4& a, const f32x4& b) { return u32x4{pk2(a[0], a[1]), pk2(a[2], a[3]), pk2(b[0], b[1]), pk2(b[2], b[3])}; }
DI u32x2 pack4(const f32x4& a) { return u32x2{pk2(a[0], a[1]), pk2(a[2], a[3])}; }
DI void mfma_done() { asm volatile("s_nop 15" ::: "memory"); }
DI float qmax(float x) {
  auto a = __builtin_amdgcn_permlane32_swap(__float_as_uint(x), __float_as_uint(x), false, false);
  x = fmaxf(__uint_as_float(a[0]), __uint_as_float(a[1]));
  auto b = __builtin_amdgcn_permlane16_swap(__float_as_uint(x), __float_as_uint(x), false, false);
  return fmaxf(__uint_as_float(b[0]), __uint_as_float(b[1]));
}
DI float qsum(float x) {
  auto a = __builtin_amdgcn_permlane32_swap(__float_as_uint(x), __float_as_uint(x), false, false);
  x = __uint_as_float(a[0]) + __uint_as_float(a[1]);
  auto b = __builtin_amdgcn_permlane16_swap(__float_as_uint(x), __float_as_uint(x), false, false);
  return __uint_as_float(b[0]) + __uint_as_float(b[1]);
}
DI float rsum16(float x) {
  x += __uint_as_float(__builtin_amdgcn_update_dpp(0u, __float_as_uint(x), 0x128, 0xf, 0xf, false));
  x += __uint_as_float(__builtin_amdgcn_update_dpp(0u, __float_as_uint(x), 0x124, 0xf, 0xf, false));
  x += __uint_as_float(__builtin_amdgcn_update_dpp(0u, __float_as_uint(x), 0x122, 0xf, 0xf, false));
  x += __uint_as_float(__builtin_amdgcn_update_dpp(0u, __float_as_uint(x), 0x121, 0xf, 0xf, false));
  return x;
}
constexpr unsigned OOB = 0x7ff00000u;
constexpr int SPIN_CAP = 1 << 18;
template <int NX, int NY> DI void zero(f32x4 (&a)[NX][NY]) {
#pragma unroll
  for (int i = 0; i < NX; i++)
#pragma unroll
    for (int j = 0; j < NY; j++) a[i][j] = f32x4{0, 0, 0, 0};
}
template <int NX, int NY> DI void pin2(f32x4 (&a)[NX][NY]) {
#pragma unroll
  for (int i = 0; i < NX; i++)
#pragma unroll
    for (int j = 0; j < NY; j++) pin(a[i][j]);
}

DI void publish(char* ws, int flag, int lane) {
  wait_vm<0>();
  __builtin_amdgcn_raw_buffer_store_b32(1u, mk_rsrc(ws + O_FL, FL_N * 128), lane == 0 ? 0u : OOB, flag * 128, SC1);
}
template <int NAP = 6> DI void poll(const char* ws, int flag_of_lane) {
  GAS const unsigned* p = (GAS const unsigned*)(ws + O_FL) + flag_of_lane * 32;
#pragma unroll 1
  for (int spin = 0; spin < SPIN_CAP; spin++) {
    unsigned v = __hip_atomic_load(p, __ATOMIC_RELAXED, __HIP_MEMORY_SCOPE_AGENT);
    if (__all(v == 1u)) break;
    __builtin_amdgcn_s_sleep(NAP);
  }
  asm volatile("" ::: "memory"); SB();
}

DI bool ready(const char* ws, int flag_of_lane) {
  unsigned v = __hip_atomic_load((GAS const unsigned*)(ws + O_FL) + flag_of_lane * 32, __ATOMIC_RELAXED, __HIP_MEMORY_SCOPE_AGENT);
  bool ok = __all(v == 1u);
  asm volatile("" ::: "memory"); SB();
  return ok;
}

template <int NT, int KS, int D, bool PERG, int START, int WRAP, int SWAP0, bool SCALE, int AUX, class PRE>
DI void f_consumer(rsrc_t ra, unsigned a0, unsigned a1, unsigned lp, int pb, f32x4 (&acc)[2][NT], unsigned tab, PRE&& pre, char* ws = nullptr, int sb = -1) {
  constexpr int U = D / 2, NB = KS / D, SLOT = NT * 2048, REG = U * SLOT;
  static_assert(KS % D == 0, "");
  u32x4 a[D][2];
  unsigned sa = pb * D * BLK;
#pragma unroll
  for (int d = 0; d < D; d++) { a[d][0] = ld128<AUX>(ra, a0, sa + d * BLK); SB(); a[d][1] = ld128<AUX>(ra, a1, sa + d * BLK); SB(); }
  u32x4 b0[NT], b1[NT], sh[2], shn[2];
  auto scale = [&](u32x4& v, int j, unsigned s) { asm volatile("v_pk_sub_u16 %0, %0, %1 clamp" : "+v"(v[j]) : "v"(s)); };
  pre();
  if constexpr (SCALE) {
    dsr128<0>(sh[0], tab); dsr128<1024>(sh[1], tab);
    wait_lgkm<0>(); pin(sh[0]); pin(sh[1]);
#pragma unroll
    for (int j = 0; j < 4; j++) { scale(a[0][0], j, sh[0][0]); scale(a[0][1], j, sh[1][0]); }
  }
  auto half = [&](u32x4 (&av)[2], u32x4 (&bc)[NT], u32x4 (&bn)[NT], unsigned addr, auto off, unsigned so, auto nx,
                  u32x4 (&an)[2], unsigned s0, unsigned s1) {
    sfor<0, NT>([&](auto i) {
      constexpr int I = decltype(i)::value, WS = NT == 4 ? 2 : 3;
      if constexpr (I % WS == 0) { constexpr int need = (I + WS < NT ? I + WS : NT); wait_lgkm<NT - need + I + decltype(nx)::value>(); }
      if constexpr (I >= SWAP0) mfma(acc[0][I], av[0], bc[I]); else mfma(acc[0][I], bc[I], av[0]);
      dsr128<I * 2048 + decltype(off)::value>(bn[I], addr);
      if constexpr (I == NT - 1) { SB(); av[0] = ld128<AUX>(ra, a0, so); SB(); }
      if constexpr (I >= SWAP0) mfma(acc[1][I], av[1], bc[I]); else mfma(acc[1][I], bc[I], av[1]);
      if constexpr (I == NT - 1) { SB(); av[1] = ld128<AUX>(ra, a1, so); SB(); }
      if constexpr (SCALE && I >= 1 && I <= 4) { scale(an[(I - 1) >> 1], ((I - 1) & 1) * 2, I < 3 ? s0 : s1); scale(an[(I - 1) >> 1], ((I - 1) & 1) * 2 + 1, I < 3 ? s0 : s1); }
    });
  };
  pin2(acc);
  SB();
  bar();
  if (sb >= 0) STAMP(1, sb);
  unsigned cur = lp + START; int reg = START;
  sfor<0, NT>([&](auto i) { dsr128<decltype(i)::value * 2048>(b0[decltype(i)::value], cur); });
#pragma unroll 1
  for (int it = 0; it < NB; it++) {
    bool last = it == NB - 1;
    pb = pb + 1 == NB ? 0 : pb + 1;
    unsigned sn = last ? OOB : pb * D * BLK;
    reg = reg + REG == WRAP ? 0 : reg + REG;
    unsigned nxt = lp + reg;
    if constexpr (SCALE) { tab += 16; dsr128<0>(shn[0], tab); dsr128<1024>(shn[1], tab); }
    sfor<0, U>([&](auto uu) {
      constexpr int u = decltype(uu)::value;
      half(a[2 * u], b0, b1, cur, ic<u * SLOT + 1024>{}, sn + 2 * u * BLK,
           ic<(SCALE && u == 0) ? 2 : 0>{}, a[2 * u + 1], sh[0][u & 3], sh[1][u & 3]);
      if constexpr (u < U - 1) {
        if constexpr (PERG) bar();
        half(a[2 * u + 1], b1, b0, cur, ic<(u + 1) * SLOT>{}, sn + (2 * u + 1) * BLK, ic<0>{}, a[2 * u + 2], sh[0][(u + 1) & 3], sh[1][(u + 1) & 3]);
      } else {
        if (!last) bar();
        if (sb >= 0 && it < 3) STAMP(1, sb + 1 + it);
        if constexpr (SCALE) { pin(shn[0]); pin(shn[1]); }
        half(a[2 * u + 1], b1, b0, nxt, ic<0>{}, sn + (2 * u + 1) * BLK, ic<0>{}, a[0], shn[0][0], shn[1][0]);
      }
    });
    if constexpr (SCALE) { sh[0] = shn[0]; sh[1] = shn[1]; }
    cur = nxt;
  }
  wait_lgkm<0>();
  mfma_done();
  pin2(acc);
}
template <int NT, int KS, int AUX> struct FSrc {
  rsrc_t r; unsigned v[NT]; int rot;
  DI void init(rsrc_t rb, unsigned boff, int rot_, int lane) {
    r = rb; rot = rot_;
#pragma unroll
    for (int n = 0; n < NT; n++) v[n] = boff + lane * 16 + n * KS * BLK - (n & 1) * 2048;
  }
  DI void issue(int g, LDS char* dst) const {
    int gg = g + rot; gg = gg >= KS / 2 ? gg - KS / 2 : gg;
    unsigned so = gg * 2048;
    sfor<0, NT>([&](auto i) {
      constexpr int n = decltype(i)::value;
      dma16<(n & 1) * 2048, AUX>(r, dst + (n >> 1) * 4096, v[n], so);
      dma16<(n & 1) * 2048 + 1024, AUX>(r, dst + (n >> 1) * 4096, v[n], so);
    });
  }
};

enum { KC = 0, TR = 1, FL = 2 };
template <int NP> struct RSrc {
  rsrc_t r; unsigned v[NP]; unsigned kstep;
  DI void kc(unsigned base, unsigned stride, int lane) {
#pragma unroll
    for (int p = 0; p < NP; p++) { int row = 8 * p + (lane >> 3); v[p] = base + row * stride + (((lane & 7) ^ ((row >> 1) & 7)) << 4) - (p & 3) * 1024; }
    kstep = 128;
  }
  DI void tr(unsigned base, unsigned stride, int lane) {
#pragma unroll
    for (int p = 0; p < NP; p++) {
      int row = 8 * (p & 7) + (lane >> 3), f = ((row >> 1) & 1) | (((row >> 3) & 1) << 1);
      v[p] = base + (p >> 3) * 128 + row * stride + (((lane & 7) ^ (f << 1)) << 4) - (p & 3) * 1024;
    }
    kstep = 64 * stride;
  }
  DI void fl(unsigned base, int lane) {
#pragma unroll
    for (int p = 0; p < NP; p++) v[p] = base + (p >> 1) * KD * BLK + (p & 1) * BLK + lane * 16 - (p & 3) * 1024;
    kstep = 2048;
  }
  DI void issue(int gg, LDS char* dst) const {
    unsigned so = gg * kstep;
    sfor<0, NP>([&](auto i) { constexpr int p = decltype(i)::value; dma16<(p & 3) * 1024>(r, dst + (p >> 2) * 4096, v[p], so); });
  }
};
template <int NP, class F> DI void r_loader(F&& iss, int rot) {
  constexpr int G = 18;
  auto issue = [&](int g, int slot) { int gg = g + rot; iss(gg >= G ? gg - G : gg, slot); };
  issue(0, 0); issue(1, 1);
  wait_vm<NP>(); bar();
  issue(2, 2); issue(3, 3);
  int slot = 0;
#pragma unroll 1
  for (int g = 1; g < G - 3; g++) {
    wait_vm<2 * NP>(); bar();
    issue(g + 3, slot); slot = (slot + 1) & 3;
  }
  wait_vm<2 * NP>(); bar();
  wait_vm<NP>(); bar();
  wait_vm<0>(); bar();
}
template <int KIND, int NTL> struct Frag {
  unsigned base[KIND == TR ? NTL : (KIND == KC ? 2 : 1)];
  DI void init(unsigned lds0, int ioff, int t0, int lane) {
    int r = lane & 15, q = lane >> 4;
    if constexpr (KIND == KC) {
      unsigned b = lds0 + ioff + (t0 * 16 + r) * 128;
      base[0] = b + ((q ^ ((r >> 1) & 7)) << 4); base[1] = b + (((4 + q) ^ ((r >> 1) & 7)) << 4);
    } else if constexpr (KIND == TR) {
      int qq = r >> 2, p = r & 3, f = ((qq >> 1) & 1) | ((q & 1) << 1);
#pragma unroll
      for (int c = 0; c < NTL; c++) { int t = t0 + c; base[c] = lds0 + ioff + (t >> 2) * 8192 + (8 * q + qq) * 128 + (((t & 3) ^ f) << 5) + p * 8; }
    } else base[0] = lds0 + ioff + t0 * 2048 + lane * 16;
  }
  template <int H, int I> DI void read(u32x4& d, unsigned so) const {
    if constexpr (KIND == KC) dsr128<I * 2048>(d, base[H] + so);
    else if constexpr (KIND == FL) dsr128<I * 2048 + H * 1024>(d, base[0] + so);
    else {
      u32x2 lo, hi;
      dsr_tr<H * 4096>(lo, base[I] + so); dsr_tr<H * 4096 + 512>(hi, base[I] + so);
      d = u32x4{lo[0], lo[1], hi[0], hi[1]};
    }
  }
};
template <int S, int N, int A = 0, int B = -1> struct Ring {
  static constexpr unsigned first = A;
  static DI unsigned next(unsigned o) { o += S; if constexpr (B >= 0) return o == A + S * N ? 0 : o == B ? A : o; else return o == A + S * N ? A : o; }
};
template <class RX, class RY, int KX, int NX, int KY, int NY>
DI void r_consumer(const Frag<KX, NX>& fx, const Frag<KY, NY>& fy, f32x4 (&acc)[NX][NY]) {
  unsigned sx = RX::first, sy = RY::first;
  auto adv = [&]() { sx = RX::next(sx); if constexpr (std::is_same<RX, RY>::value) sy = sx; else sy = RY::next(sy); };
  u32x4 xa[2][NX], ya[2][NY], xb[2][NX], yb[2][NY];
  auto rd = [&](u32x4 (&xn)[2][NX], u32x4 (&yn)[2][NY], auto i) {
    constexpr int I = decltype(i)::value, H = I / (NX + NY), J = I % (NX + NY);
    if constexpr (J < NY) fy.template read<H, J>(yn[H][J], sy); else fx.template read<H, J - NY>(xn[H][J - NY], sx);
  };
  auto land = [&](u32x4 (&xn)[2][NX], u32x4 (&yn)[2][NY]) {
    wait_lgkm<0>();
#pragma unroll
    for (int h = 0; h < 2; h++) {
#pragma unroll
      for (int i = 0; i < NX; i++) pin(xn[h][i]);
#pragma unroll
      for (int i = 0; i < NY; i++) pin(yn[h][i]);
    }
  };
  auto step = [&](u32x4 (&xc)[2][NX], u32x4 (&yc)[2][NY], u32x4 (&xn)[2][NX], u32x4 (&yn)[2][NY], auto more) {
    adv();
    sfor<0, 2 * NX * NY>([&](auto i) {
      constexpr int I = decltype(i)::value, H = I / (NX * NY), J = I % (NX * NY);
      mfma(acc[J / NY][J % NY], xc[H][J / NY], yc[H][J % NY]);
      if constexpr (decltype(more)::value && I < 2 * (NX + NY)) rd(xn, yn, i);
    });
    if constexpr (decltype(more)::value) sfor<2 * NX * NY, 2 * (NX + NY)>([&](auto i) { rd(xn, yn, i); });
    if constexpr (decltype(more)::value) land(xn, yn);
  };
  pin2(acc);
  SB();
  bar();
  sfor<0, 2 * (NX + NY)>([&](auto i) { rd(xa, ya, i); });
  land(xa, ya);
#pragma unroll 1
  for (int g = 0; g < 8; g++) {
    bar();
    step(xa, ya, xb, yb, ic<1>{});
    bar();
    step(xb, yb, xa, ya, ic<1>{});
  }
  bar();
  step(xa, ya, xb, yb, ic<1>{});
  step(xb, yb, xa, ya, ic<0>{});
  mfma_done();
  pin2(acc);
}
DI u64 mix64(u64 z) { z += 0x9E3779B97F4A7C15ull; z = (z ^ (z >> 30)) * 0xBF58476D1CE4E5B9ull; z = (z ^ (z >> 27)) * 0x94D049BB133111EBull; return z ^ (z >> 31); }
#endif

constexpr int KF_CODE = 48 * 1024, K0_CODE = 32 * 1024;
constexpr int R0 = 98304, L_TAB = 147456, L_SCR = L_TAB + 8192 + 64, KF_LDS = L_SCR + 1024;
extern "C" __global__ __launch_bounds__(320) void kf_main(const char* x, const char* wqc, const char* woc, char* ws, char* out) {
#if defined(__HIP_DEVICE_COMPILE__)
  extern __shared__ __attribute__((aligned(16))) char shm[];
  STAMP(1, 0);
  const int bid = blockIdx.x, xcd = bid & 7, idx = bid >> 3;
  const int wid = __builtin_amdgcn_readfirstlane(threadIdx.x >> 6), lane = threadIdx.x & 63, r = lane & 15, q = lane >> 4;
  LDS char* sm = (LDS char*)shm;
  const unsigned l0 = (unsigned)(size_t)sm;
  const int b = xcd >> 2;
  const int cs = (xcd & 3) + 4 * (idx >> 3), rp2 = b * 8 + (idx & 7), ph2 = PH2;
  const bool isT = cs < 9;
  const int rp = 2 * xcd + (idx & 1), kg = idx >> 1, ph3 = kg % 3;
  const int rp4 = b * 8 + (xcd & 2) * 2 + (idx & 3), dp = (xcd & 1) * 6 + (idx >> 2), ph4 = (idx >> 2) & 3;
  const int so = (idx - 24) * 8 + xcd;
  const int rt5 = xcd * 16 + (idx >> 1), hf = idx & 1;
  constexpr int TS = 22528, TW = 10240;
  const int sb = so & 1, sp = so < 24 ? so >> 1 : (so - 24) >> 1;

  if (wid == 4) {
    FSrc<4, KD, 0> h; h.init(mk_rsrc(ws + O_H, SZ_ACT), (b * 64 + kg * 4) * KD * BLK, ph3 * 6, lane);
    {
      FSrc<9, KD, 0> w; w.init(mk_rsrc(ws + O_W, 144 * KD * BLK), (isT ? cs * 8 : 72 + (cs - 9) * 9) * KD * BLK, ph2 * 3, lane);
      if (isT) w.v[8] = (135 + cs) * KD * BLK + lane * 16;
      w.issue(0, sm); w.issue(1, sm + 18432);
      wait_vm<18>(); bar();
      w.issue(2, sm + 2 * 18432); w.issue(3, sm + 3 * 18432);
      int slot = 4;
#pragma unroll 1
      for (int g = 1; g < 15; g++) {
        wait_vm<36>(); bar();
        w.issue(g + 3, sm + slot * 18432); slot = slot == 5 ? 0 : slot + 1;
      }
      wait_vm<36>(); bar();
#pragma unroll 1
      for (int g = 2; g < 6; g++) h.issue(g, sm + R0 + g * 8192);
      wait_vm<50>(); bar();
#pragma unroll 1
      for (int g = 6; g < 10; g++) h.issue(g, sm + (g - 6) * 8192);
      wait_vm<63>(); bar();
#pragma unroll 1
      for (int g = 10; g < 14; g++) h.issue(g, sm + (g - 6) * 8192);
      if (KFW) CODE_WARM(kf_main, KF_CODE, idx, l0 + L_SCR);
      bar();
      if (HMODE == 1) __builtin_amdgcn_s_sleep(HSLEEP);
      h.issue(0, sm + R0); h.issue(1, sm + R0 + 8192);
#pragma unroll 1
      for (int g = 14; g < 18; g++) h.issue(g, sm + (g - 6) * 8192);
      wait_vm<0>(); STAMPL(21); bar();
      bar();
      if (KFW) CODE_WARM(kf_main, KF_CODE, idx, l0 + L_SCR);
    }
    if (idx < 24) {
      auto fv = [&](int j) { int t = dp * 6 + 2 * j + (lane >> 5); return FL_V + ((t < 63 ? 9 + t / 9 : t - 63) * 16 + b * 8) * 4 + (lane & 31); };
      FSrc<6, 32, SC1> v; v.init(mk_rsrc(ws + O_V, SZ_ACT), (b * 72 + dp * 6) * 32 * BLK, ph4 * 4, lane);
      int g = 0;
      GAS const unsigned* fl = (GAS const unsigned*)(ws + O_FL);
      unsigned f0 = __hip_atomic_load(fl + fv(0) * 32, __ATOMIC_RELAXED, __HIP_MEMORY_SCOPE_AGENT), f1 = __hip_atomic_load(fl + fv(1) * 32, __ATOMIC_RELAXED, __HIP_MEMORY_SCOPE_AGENT),
               f2 = __hip_atomic_load(fl + fv(2) * 32, __ATOMIC_RELAXED, __HIP_MEMORY_SCOPE_AGENT);
      bool vr = __all((f0 & f1 & f2) == 1u);
      bar();
      if (vr) {
        __builtin_amdgcn_s_sleep(2);
#pragma unroll 1
        for (; g < 7; g++) v.issue(g, sm + (g < 4 ? R0 + g * 12288 : (g - 4) * 12288));
      }
      STAMPL(22);
      bar();
      if (g == 0) { poll(ws, fv(0)); poll(ws, fv(1)); poll(ws, fv(2)); }
#pragma unroll 1
      for (; g < 12; g++) { v.issue(g, sm + (g < 4 ? R0 + g * 12288 : (g - 4) * 12288)); wait_vm<VTHR>(); }
      wait_vm<12>();
      STAMPL(23);
      bar();
      bar();
#pragma unroll 1
      for (int g = 12; g < 15; g++) v.issue(g, sm + R0 + (g - 12) * 12288);
      unsigned xo[24];
#pragma unroll
      for (int p = 0; p < 24; p++) { int ch = p * 64 + lane, row = (ch * 5462) >> 16; xo[p] = (rp4 * 128 + row) * 2304 + dp * 192 + (ch - row * 12) * 16; pin(xo[p]); }
      wait_vm<36>(); bar();
      v.issue(15, sm + R0 + 3 * 12288);
      wait_vm<0>();
      {
        rsrc_t rx = mk_rsrc(x, 2048 * 2304);
#pragma unroll
        for (int p = 0; p < 24; p++) dma16<0>(rx, sm + p * 1024, xo[p], 0);
        dma16<0>(mk_rsrc(ws + O_MOD, 4608), sm + 24576, lane < 12 ? lane * 16 : OOB, b * 2304 + dp * 192);
      }
      bar();
      {
        poll<2>(ws, FL_W + b * 36 + dp * 3 + (lane < 3 ? lane : 0));
        rsrc_t rw = mk_rsrc(ws + O_WQC, SZ_TOK);
#pragma unroll
        for (int u = 0; u < 5; u++) {
          unsigned o = ((b * 5 + u) * KD + dp * 3) * BLK;
          dma16<0, SC1>(rw, sm + 49152 + u * 3072, lane * 16, o); dma16<1024, SC1>(rw, sm + 49152 + u * 3072, lane * 16, o); dma16<2048, SC1>(rw, sm + 49152 + u * 3072, lane * 16, o);
        }
      }
      wait_vm<0>();
      STAMPL(24);
      bar();
    } else if (so < 60) {
      RSrc<12> w; RSrc<10> t;
      t.r = mk_rsrc(ws + (so < 24 ? O_KC : O_VC), SZ_TOK); t.fl(sb * 5 * KD * BLK, lane);
      if (so < 24) { w.r = mk_rsrc(wqc, DM * 2304); w.kc(sp * 96 * 2304, 2304, lane); }
      else { w.r = mk_rsrc(woc, DM * 2304); w.tr(sp * 128, 2304, lane); }
      auto iss = [&](int g) {
        int sl = g >= 12 ? g - 12 : g >= 6 ? g - 6 : g;
        LDS char* d = sm + (sl < 2 ? R0 + sl * TS : (sl - 2) * TS);
        t.issue(g, d);
        unsigned o = g * w.kstep;
        sfor<0, 8>([&](auto i) { constexpr int p = decltype(i)::value; dma16<(p & 3) * 1024>(w.r, d + TW + (p >> 2) * 4096, w.v[p], o); });
        if (so < 24) sfor<8, 12>([&](auto i) { constexpr int p = decltype(i)::value; dma16<(p & 3) * 1024>(w.r, d + TW + 8192, w.v[p], o); });
      };
      bar();
#pragma unroll 1
      for (int g = 0; g < WEARLY; g++) iss(g);
      bar();
#pragma unroll 1
      for (int g = WEARLY; g < 4; g++) iss(g);
      wait_vm<54>(); bar();
      iss(4); iss(5);
#pragma unroll 1
      for (int g = 1; g < 18; g++) {
        if (g < 15) wait_vm<54>(); else if (g == 15) wait_vm<36>(); else if (g == 16) wait_vm<18>(); else wait_vm<0>();
        bar();
        if (g < 13) iss(g + 5);
      }
    } else {
      bar();
      bar();
    }
    return;
  }

  const unsigned lp = l0 + lane * 16;
  {
    rsrc_t ra = mk_rsrc(ws + O_H, SZ_ACT);
    unsigned a0 = (rp2 * 8 + wid * 2) * KD * BLK + lane * 16, a1 = a0 + KD * BLK;
    f32x4 acc[2][9]; zero(acc);
    rsrc_t rv = mk_rsrc(ws + O_V, SZ_ACT);
    const unsigned vb = (b * 72 * 32 + (rp2 & 7) * 4 + wid) * BLK;
    if (isT) {
      asm volatile("; T" : "+v"(a0), "+v"(a1));
      f_consumer<9, KD, 6, true, 0, 110592, 8, false, 0>(ra, a0, a1, lp, ph2, acc, 0, [] {});
      STAMP(1, 1);
      rsrc_t rt = mk_rsrc(ws + O_T, SZ_ACT);
      u32x4 o[9];
#pragma unroll
      for (int i = 0; i < 8; i++) o[i] = pack8(acc[i >> 2][2 * (i & 3)], acc[i >> 2][1 + 2 * (i & 3)]);
      o[8] = pack8(acc[0][8], acc[1][8]);
      pin_all(o);
#pragma unroll
      for (int i = 0; i < 8; i++) st128b<SC1>(o[i], rt, lane * 16 + (i & 3) * BLK, ((rp2 * 8 + wid * 2 + (i >> 2)) * KD + cs * 4) * BLK);
      SB(); bar();
      publish(ws, FL_T + (rp2 * 9 + cs) * 4 + wid, lane);
      st128<SC1>(o[8], rv, lane * 16, vb + (63 + cs) * 32 * BLK);
    } else {
      asm volatile("; V" : "+v"(a0), "+v"(a1));
      f_consumer<9, KD, 6, true, 0, 110592, 0, false, 0>(ra, a0, a1, lp, ph2, acc, 0, [] {});
      STAMP(1, 1);
      bar();
      u32x4 o[9];
#pragma unroll
      for (int n = 0; n < 9; n++) o[n] = pack8(acc[0][n], acc[1][n]);
      pin_all(o);
      asm volatile("s_sleep %0" ::"n"(VDELAY) : "memory");
#pragma unroll
      for (int n = 0; n < 9; n++) st128b<SC1>(o[n], rv, lane * 16, vb + ((cs - 9) * 9 + n) * 32 * BLK);
      st_end();
    }
    publish(ws, FL_V + (cs * 16 + rp2) * 4 + wid, lane);
    STAMP(1, 2);
  }
  {
    const int rt0 = rp * 8 + wid * 2;
    f32x4 acc[2][4]; zero(acc);
    poll(ws, FL_T + (rp * 9 + (lane < 9 ? lane : 0)) * 4 + wid);
    STAMP(1, 3);
    unsigned a0 = rt0 * KD * BLK + lane * 16, a1 = a0 + KD * BLK;
    f_consumer<4, KD, 12, false, R0, 147456, 99, false, SC1>(mk_rsrc(ws + O_T, SZ_ACT), a0, a1, lp, ph3, acc, 0, [] {}, ws, 17);
    STAMP(1, 4);
    rsrc_t rpp = mk_rsrc(ws + O_P, 128 * 32 * BLK), rs = mk_rsrc(ws + O_ST, 128 * 16 * 128);
    u32x2 rec[2]; u32x4 po[4];
#pragma unroll
    for (int t = 0; t < 2; t++) {
      float m = acc[t][0][0];
#pragma unroll
      for (int n = 0; n < 4; n++)
#pragma unroll
        for (int j = 0; j < 4; j++) m = fmaxf(m, acc[t][n][j]);
      m = ceilf(qmax(m));
      float l = 0;
#pragma unroll
      for (int n = 0; n < 4; n++)
#pragma unroll
        for (int j = 0; j < 4; j++) { float p = __builtin_amdgcn_exp2f(acc[t][n][j] - m); acc[t][n][j] = p; l += p; }
      unsigned o = ((rt0 + t) * 32 + kg * 2) * BLK;
      po[2 * t] = pack8(acc[t][0], acc[t][1]); po[2 * t + 1] = pack8(acc[t][2], acc[t][3]);
      pin(po[2 * t]); pin(po[2 * t + 1]);
      st128b<SC1>(po[2 * t], rpp, lane * 16, o); st128b<SC1>(po[2 * t + 1], rpp, lane * 16 + 1024, o);
      rec[t] = u32x2{__float_as_uint(m), __float_as_uint(qsum(l))};
    }
    pin_all(po);
    bar();
    wait_vm<0>();
    st64<SC1>(q & 1 ? rec[1] : rec[0], rs, lane < 32 ? (q & 1) * 2048 + r * 8 : OOB, (rt0 * 16 + kg) * 128);
    STAMP(1, 5);
  }
  if (idx < 24) {
    const int rt0 = rp4 * 8 + wid * 2;
    rsrc_t rs = mk_rsrc(ws + O_ST, 128 * 16 * 128);
    u32x2 sv[2][4];
    f32x4 acc[2][6]; zero(acc);
    __builtin_amdgcn_s_sleep(PDELAY);
#pragma unroll 1
    for (int spin = 0; spin < SPIN_CAP; spin++) {
      bool ok = true;
#pragma unroll
      for (int t = 0; t < 2; t++)
#pragma unroll
        for (int j = 0; j < 4; j++) { sv[t][j] = ld64<SC1>(rs, (q * 4 + j) * 128 + r * 8, (rt0 + t) * 2048); ok &= sv[t][j][1] != 0; }
      if (__all(ok)) break;
      asm volatile("s_sleep %0" ::"n"(PNAP) : "memory");
    }
    asm volatile("" ::: "memory"); SB();
    STAMP(1, 6);
    LDS unsigned* tab = (LDS unsigned*)(sm + L_TAB + wid * 2048);
    float inv[2];
    unsigned a0 = rt0 * 32 * BLK + lane * 16, a1 = a0 + 32 * BLK;
    f_consumer<6, 32, 8, false, R0, 147456, 99, true, SC1>(mk_rsrc(ws + O_P, 128 * 32 * BLK), a0, a1, lp, ph4, acc, l0 + L_TAB + wid * 2048 + r * 64, [&] {
#pragma unroll
      for (int t = 0; t < 2; t++) {
        float mg = qmax(fmaxf(fmaxf(__uint_as_float(sv[t][0][0]), __uint_as_float(sv[t][1][0])), fmaxf(__uint_as_float(sv[t][2][0]), __uint_as_float(sv[t][3][0]))));
        float l = 0; u32x4 k;
#pragma unroll
        for (int j = 0; j < 4; j++) {
          float d = fminf(mg - __uint_as_float(sv[t][j][0]), 255.f);
          l += __uint_as_float(sv[t][j][1]) * __builtin_amdgcn_exp2f(-d);
          k[j] = (unsigned)d * 0x00800080u;
        }
        *(LDS u32x4*)(tab + t * 256 + r * 16 + ((q - ph4) & 3) * 4) = k;
        inv[t] = __builtin_amdgcn_rcpf(qsum(l));
      }
      wait_lgkm<0>();
    }, ws, 13);
    bar();
    STAMP(1, 7);
    rsrc_t ro = mk_rsrc(ws + O_X1, SZ_ACT);
    u32x4 xo[2][3];
#pragma unroll
    for (int t = 0; t < 2; t++) {
#pragma unroll
      for (int n = 0; n < 6; n++) {
        u32x2 xv = *(LDS u32x2*)(sm + ((wid * 2 + t) * 16 + r) * 192 + n * 32 + q * 8), gv = *(LDS u32x2*)(sm + 24576 + n * 32 + q * 8);
        acc[t][n] = f32x4{blo(xv[0]), bhi(xv[0]), blo(xv[1]), bhi(xv[1])} + f32x4{blo(gv[0]), bhi(gv[0]), blo(gv[1]), bhi(gv[1])} * (acc[t][n] * inv[t]);
      }
#pragma unroll
      for (int p = 0; p < 3; p++) { xo[t][p] = pack8(acc[t][2 * p], acc[t][2 * p + 1]); pin(xo[t][p]); st128b<SC1>(xo[t][p], ro, lane * 16 + p * BLK, ((rt0 + t) * KD + dp * 3) * BLK); }
    }
    SB();
    {
      rsrc_t rp_ = mk_rsrc(ws + O_SP, 128 * 12 * 2560);
      f32x4 sp[2][5]; zero(sp);
#pragma unroll
      for (int u = 0; u < 5; u++)
#pragma unroll
        for (int p = 0; p < 3; p++) {
          u32x4 w = *(LDS u32x4*)(sm + 49152 + u * 3072 + p * 1024 + lane * 16);
          sp[0][u] = mfma_b(w, xo[0][p], sp[0][u]); sp[1][u] = mfma_b(w, xo[1][p], sp[1][u]);
        }
      u32x4 so4[4] = {pack8(sp[0][0], sp[0][1]), pack8(sp[0][2], sp[0][3]), pack8(sp[1][0], sp[1][1]), pack8(sp[1][2], sp[1][3])};
      u32x2 h0 = pack4(sp[0][4]), h1 = pack4(sp[1][4]);
      unsigned o = (rt0 * 12 + dp) * 2560;
      st64<SC1>(h0, rp_, lane * 8 + 2048, o); st64<SC1>(h1, rp_, lane * 8 + 2048, o + 30720);
      pin_all(so4);
      st128b<SC1>(so4[0], rp_, lane * 16, o); st128b<SC1>(so4[1], rp_, lane * 16 + 1024, o);
      st128b<SC1>(so4[2], rp_, lane * 16, o + 30720); st128b<SC1>(so4[3], rp_, lane * 16 + 1024, o + 30720);
      st_end();
    }
    u32x2 rec[2];
#pragma unroll
    for (int t = 0; t < 2; t++) {
      f32x2 s1 = {0, 0}, s2 = {0, 0};
#pragma unroll
      for (int p = 0; p < 3; p++)
#pragma unroll
        for (int j = 0; j < 4; j++) s1 += f32x2{blo(xo[t][p][j]), bhi(xo[t][p][j])};
      float mw = qsum(s1[0] + s1[1]) * (1.f / 96);
#pragma unroll
      for (int p = 0; p < 3; p++)
#pragma unroll
        for (int j = 0; j < 4; j++) { f32x2 d = f32x2{blo(xo[t][p][j]), bhi(xo[t][p][j])} - mw; s2 += d * d; }
      rec[t] = u32x2{__float_as_uint(mw), __float_as_uint(qsum(s2[0] + s2[1])) | 0x80000000u};
    }
    wait_vm<0>();
    st64<SC1>(q & 1 ? rec[1] : rec[0], mk_rsrc(ws + O_XS, 128 * 12 * 128), lane < 32 ? (q & 1) * 1536 + r * 8 : OOB, (rt0 * 12 + dp) * 128);
  } else if (so < 24) {
    if (wid < 3) {
      Frag<FL, 5> ft; ft.init(l0, 0, 0, lane);
      Frag<KC, 2> fw; fw.init(l0, TW, wid * 2, lane);
      f32x4 acc[2][5]; zero(acc);
      r_consumer<Ring<TS, 2, R0, 4 * TS>, Ring<TS, 2, R0, 4 * TS>>(fw, ft, acc);
      STAMP(1, 7);
      rsrc_t ro = mk_rsrc(ws + O_WQC, SZ_TOK), rc = mk_rsrc(ws + O_CS, 2 * 36 * 320);
#pragma unroll
      for (int t = 0; t < 5; t++) {
        u32x4 w = pack8(acc[0][t] * QK_SCALE, acc[1][t] * QK_SCALE);
        st128<SC1>(w, ro, lane * 16, ((sb * 5 + t) * KD + sp * 3 + wid) * BLK);
        float c = qsum(((blo(w[0]) + bhi(w[0])) + (blo(w[1]) + bhi(w[1]))) + ((blo(w[2]) + bhi(w[2])) + (blo(w[3]) + bhi(w[3]))));
        __builtin_amdgcn_raw_buffer_store_b32(__float_as_uint(c), rc, lane < 16 ? lane * 4 : OOB, (sb * 36 + sp * 3 + wid) * 320 + t * 64, SC1);
      }
      publish(ws, FL_W + sb * 36 + sp * 3 + wid, lane);
    } else {
#pragma unroll 1
      for (int g = 0; g < 18; g++) bar();
    }
  } else if (so < 60) {
    Frag<FL, 5> ft; ft.init(l0, 0, 0, lane);
    Frag<TR, 1> fw; fw.init(l0, TW, wid, lane);
    f32x4 acc[5][1]; zero(acc);
    r_consumer<Ring<TS, 2, R0, 4 * TS>, Ring<TS, 2, R0, 4 * TS>>(ft, fw, acc);
    STAMP(1, 7);
    rsrc_t ro = mk_rsrc(ws + O_VOC, SZ_TOK);
    unsigned o = (sb * 72 + sp * 4 + wid) * 2560;
    st128<SC1>(pack8(acc[0][0], acc[1][0]), ro, lane * 16, o);
    st128<SC1>(pack8(acc[2][0], acc[3][0]), ro, lane * 16 + 1024, o);
    st64<SC1>(pack4(acc[4][0]), ro, lane * 8 + 2048, o);
    publish(ws, FL_O + sb * 72 + sp * 4 + wid, lane);
  }
  STAMP(1, 8);
  {
    rsrc_t rx = mk_rsrc(ws + O_X1, SZ_ACT), rv = mk_rsrc(ws + O_VOC, SZ_TOK), rc = mk_rsrc(ws + O_CS, 2 * 36 * 320), rp_ = mk_rsrc(ws + O_SP, 128 * 12 * 2560);
    u32x4 vf[9][2]; u32x2 vh[9], xr[9];
    const int dt0 = hf * 36 + wid * 9;
    poll(ws, lane < 36 ? FL_W + b * 36 + lane : FL_O + b * 72 + dt0 + (lane < 45 ? lane - 36 : 0));
    STAMP(1, 9);
    f32x4 cs[5];
    {
      u32x4 c[3][5];
#pragma unroll
      for (int j = 0; j < 3; j++)
#pragma unroll
        for (int t = 0; t < 5; t++) c[j][t] = ld128<SC1>(rc, r + 16 * j < 36 ? (r + 16 * j) * 320 + q * 16 : OOB, b * 36 * 320 + t * 64);
      SB();
#pragma unroll
      for (int n = 0; n < 9; n++) {
        unsigned o = (b * 72 + dt0 + n) * 2560;
        vf[n][0] = ld128<SC1>(rv, lane * 16, o); vf[n][1] = ld128<SC1>(rv, lane * 16 + 1024, o); vh[n] = ld64<SC1>(rv, lane * 8 + 2048, o);
      }
      SB();
#pragma unroll
      for (int t = 0; t < 5; t++) {
        cs[t] = __builtin_bit_cast(f32x4, c[0][t]) + __builtin_bit_cast(f32x4, c[1][t]) + __builtin_bit_cast(f32x4, c[2][t]);
#pragma unroll
        for (int j = 0; j < 4; j++) cs[t][j] = rsum16(cs[t][j]);
        pin(cs[t]);
      }
    }
    u32x4 id[2];
    { unsigned one = q == (r >> 2) ? ((r & 1) ? 0x3f800000u : 0x00003f80u) : 0u, lo = (r & 2) ? 0u : one, hi = (r & 2) ? one : 0u;
      id[0] = u32x4{lo, hi, 0u, 0u}; id[1] = u32x4{0u, 0u, lo, hi}; }
    STAMP(1, 26);
    u32x2 xs[3];
    rsrc_t rs = mk_rsrc(ws + O_XS, 128 * 12 * 128);
#pragma unroll 1
    for (int spin = 0; spin < SPIN_CAP; spin++) {
      bool ok = true;
#pragma unroll
      for (int j = 0; j < 3; j++) { xs[j] = ld64<SC1>(rs, (q * 3 + j) * 128 + r * 8, rt5 * 1536); ok &= (int)xs[j][1] < 0; }
      if (__all(ok)) break;
      asm volatile("s_sleep %0" ::"n"(PNAP) : "memory");
    }
    asm volatile("" ::: "memory"); SB();
    STAMP(1, 10);
    u32x4 pf[3][2]; u32x2 ph[3];
#pragma unroll
    for (int j = 0; j < 3; j++) {
      unsigned o = (rt5 * 12 + wid * 3 + j) * 2560;
      pf[j][0] = ld128<SC1>(rp_, lane * 16, o); pf[j][1] = ld128<SC1>(rp_, lane * 16 + 1024, o); ph[j] = ld64<SC1>(rp_, lane * 8 + 2048, o);
    }
#pragma unroll
    for (int n = 0; n < 9; n++) xr[n] = ld64<SC1>(rx, lane * 16 + ((dt0 + n) & 1) * 8, (rt5 * KD + ((dt0 + n) >> 1)) * BLK);
    SB();
    float mean = qsum(__uint_as_float(xs[0][0]) + __uint_as_float(xs[1][0]) + __uint_as_float(xs[2][0])) * (1.f / 12), m2 = 0;
#pragma unroll
    for (int j = 0; j < 3; j++) { float d = __uint_as_float(xs[j][0]) - mean; m2 += __uint_as_float(xs[j][1] & 0x7fffffffu) + 96.f * d * d; }
    float rstd = __builtin_amdgcn_rsqf(qsum(m2) * (1.f / DM) + 1e-5f);
#pragma unroll
    for (int t = 0; t < 5; t++) { cs[t] *= -mean * rstd; pin(cs[t]); }
    f32x4 sc[5] = {};
#pragma unroll
    for (int j = 0; j < 3; j++) {
      sc[0] = mfma_b(id[0], pf[j][0], sc[0]); sc[1] = mfma_b(id[1], pf[j][0], sc[1]);
      sc[2] = mfma_b(id[0], pf[j][1], sc[2]); sc[3] = mfma_b(id[1], pf[j][1], sc[3]);
      sc[4] = mfma_b(id[0], u32x4{ph[j][0], ph[j][1], 0u, 0u}, sc[4]);
    }
    LDS char* rl = sm + R0 + lane * 16;
#pragma unroll
    for (int t = 0; t < 5; t++) *(LDS f32x4*)(rl + (wid * 5 + t) * 1024) = sc[t];
    wait_lgkm<0>(); bar();
    STAMP(1, 11);
#pragma unroll
    for (int t = 0; t < 5; t++)
      sc[t] = ((*(LDS f32x4*)(rl + t * 1024) + *(LDS f32x4*)(rl + (5 + t) * 1024)) + (*(LDS f32x4*)(rl + (10 + t) * 1024) + *(LDS f32x4*)(rl + (15 + t) * 1024))) * rstd + cs[t];
#pragma unroll
    for (int j = 1; j < 4; j++) sc[4][j] = q == 3 ? -__builtin_inff() : sc[4][j];
    float m = sc[0][0];
#pragma unroll
    for (int t = 0; t < 5; t++)
#pragma unroll
      for (int j = 0; j < 4; j++) m = fmaxf(m, sc[t][j]);
    m = qmax(m);
    float l = 0;
#pragma unroll
    for (int t = 0; t < 5; t++)
#pragma unroll
      for (int j = 0; j < 4; j++) { float p = __builtin_amdgcn_exp2f(sc[t][j] - m); sc[t][j] = p; l += p; }
    float inv = __builtin_amdgcn_rcpf(qsum(l));
    u32x4 p0 = pack8(sc[0] * inv, sc[1] * inv), p1 = pack8(sc[2] * inv, sc[3] * inv);
    u32x2 p2h = pack4(sc[4] * inv);
    u32x4 p2 = {p2h[0], p2h[1], 0u, 0u};
    STAMP(1, 27);
    rsrc_t ro = mk_rsrc(out, 2048 * 2304);
    u32x2 ov[9];
#pragma unroll
    for (int n = 0; n < 9; n++) {
      f32x4 o = {blo(xr[n][0]), bhi(xr[n][0]), blo(xr[n][1]), bhi(xr[n][1])};
      o = mfma_b(vf[n][0], p0, o);
      o = mfma_b(vf[n][1], p1, o);
      o = mfma_b(u32x4{vh[n][0], vh[n][1], 0u, 0u}, p2, o);
      ov[n] = pack4(o);
    }
    unsigned orow = (rt5 * 16 + r) * 2304;
    u32x4 oo[4];
#pragma unroll
    for (int n = 0; n < 8; n += 2) {
      auto e = __builtin_amdgcn_permlane16_swap(ov[n][0], ov[n + 1][0], false, false);
      auto f = __builtin_amdgcn_permlane16_swap(ov[n][1], ov[n + 1][1], false, false);
      oo[n >> 1] = u32x4{e[0], f[0], e[1], f[1]};
    }
    pin_all(oo);
#pragma unroll
    for (int n = 0; n < 4; n++) st128b(oo[n], ro, orow + (q & 1) * 32 + (q >> 1) * 16, (dt0 + 2 * n) * 32);
    st_end();
    st64(ov[8], ro, orow + q * 8, (dt0 + 8) * 32);
  }
  ENDSTAMP(1, 12);
#endif
}

constexpr int K0_SCR = 147456 + 9216 + 2304, K0_LDS = K0_SCR + 1024;
extern "C" __global__ __launch_bounds__(384) void k0_pre(const char* x, const char* c, const char* y, const char* wqkv, const char* wo,
                                                        const char* wqc, const char* wkvc, const char* woc, const char* wmod, char* ws) {
#if defined(__HIP_DEVICE_COMPILE__)
  extern __shared__ __attribute__((aligned(16))) char shm[];
  STAMP(0, 0);
  int bid = blockIdx.x, xcd = bid & 7, idx = bid >> 3;
  int wid = __builtin_amdgcn_readfirstlane(threadIdx.x >> 6), lane = threadIdx.x & 63;
  int qd = xcd & 3, nr = (qd & 1) ? 4 : 5, nc = (qd & 2) ? 4 : 5, r0 = (qd & 1) ? 5 : 0, c0 = (qd & 2) ? 5 : 0;
  LDS char* sm = (LDS char*)shm;
  unsigned l0 = (unsigned)(size_t)sm;
  int r = lane & 15, q = lane >> 4;
  if (wid == 0) CODE_WARM(k0_pre, K0_CODE, idx, l0 + K0_SCR);
  if (idx < nr * nc) {
    int ti = r0 + idx % nr, tj = c0 + idx / nr, mat = xcd >> 2;
    int rot = (18 * ((ti + tj) % K0PH) + K0PH / 2) / K0PH + K0XO * ((qd ^ (qd >> 1)) & 1);
    constexpr int SLOT = 32768;
    if (wid >= 4) {
      RSrc<16> s;
      if (wid == 4) { s.r = mk_rsrc(wqkv, DM * 6912); s.kc(ti * 128 * 6912 + mat * 4608, 6912, lane); }
      else if (mat == 0) { s.r = mk_rsrc(wqkv, DM * 6912); s.kc(tj * 128 * 6912 + 2304, 6912, lane); }
      else { s.r = mk_rsrc(wo, DM * 2304); s.tr(tj * 256, 2304, lane); }
      LDS char* d = sm + (wid - 4) * 16384;
      r_loader<16>([&](int g, int slot) { s.issue(g, d + slot * SLOT); }, rot);
    } else {
      int wi = wid >> 1, wj = wid & 1;
      f32x4 acc[4][4]; zero(acc);
      Frag<KC, 4> fx; fx.init(l0, 0, wi * 4, lane);
      float sc = 1.f;
      if (mat == 0) { Frag<KC, 4> fy; fy.init(l0, 16384, wj * 4, lane); r_consumer<Ring<SLOT, 4>, Ring<SLOT, 4>>(fx, fy, acc); sc = QK_SCALE; }
      else { Frag<TR, 4> fy; fy.init(l0, 16384, wj * 4, lane); r_consumer<Ring<SLOT, 4>, Ring<SLOT, 4>>(fx, fy, acc); }
      STAMP(0, 2); STAMP(0, 8);
      rsrc_t ro = mk_rsrc(ws + O_W, 144 * KD * BLK);
      unsigned o = ((mat * 72 + tj * 8 + wj * 4) * KD + ti * 4 + wi * 2) * BLK;
      u32x4 ov[8];
#pragma unroll
      for (int i = 0; i < 8; i++) ov[i] = pack8(acc[2 * (i & 1)][i >> 1] * sc, acc[2 * (i & 1) + 1][i >> 1] * sc);
      pin_all(ov);
#pragma unroll
      for (int i = 0; i < 8; i++) st128b<K0AUX>(ov[i], ro, lane * 16 + (i & 1) * 1024, o + (i >> 1) * KD * BLK);
      if (wid == 0) CODE_WARM(kf_main, KF_CODE, idx, l0 + K0_SCR);
      ENDSTAMP(0, 3);
    }
    return;
  }
  int o = (xcd >> 2) * 47 + (qd == 0 ? 0 : qd == 1 ? 7 : qd == 2 ? 19 : 31) + idx - nr * nc;
  if (o < 72 && !(o & 1)) {
    constexpr int YS = 20480, WOFF = 3 * YS;
    int pn = o >> 1, rot = (18 * (pn % K0PH) + K0PH / 2) / K0PH;
    auto wrap = [&](int g) { g += rot; return g >= 18 ? g - 18 : g; };
    if (wid == 4) {
      RSrc<20> s; s.r = mk_rsrc(y, 154 * 2304); s.kstep = 128;
#pragma unroll
      for (int p = 0; p < 20; p++) {
        int row = 8 * p + (lane >> 3), b = row >= 80, t = row - 80 * b;
        s.v[p] = t < 77 ? (b * 77 + t) * 2304 + (((lane & 7) ^ ((row >> 1) & 7)) << 4) - (p & 3) * 1024 : OOB;
      }
      s.issue(wrap(0), sm); s.issue(wrap(1), sm + YS); s.issue(wrap(2), sm + 2 * YS);
      wait_vm<40>(); bar();
      int slot = 0;
#pragma unroll 1
      for (int g = 1; g < 16; g++) { wait_vm<20>(); bar(); s.issue(wrap(g + 2), sm + slot * YS); slot = slot == 2 ? 0 : slot + 1; }
      wait_vm<20>(); bar();
      wait_vm<0>(); bar();
    } else if (wid == 5) {
      RSrc<8> s; s.r = mk_rsrc(wkvc, DM * 4608); s.tr(pn * 128, 4608, lane);
#pragma unroll 1
      for (int g = 0; g < 12; g++) s.issue(wrap(g), sm + WOFF + g * 8192);
      wait_vm<48>(); bar();
#pragma unroll 1
      for (int g = 1; g < 7; g++) { if (g == 6) wait_vm<40>(); bar(); s.issue(wrap(g + 11), sm + WOFF + (g - 1) * 8192); }
#pragma unroll 1
      for (int g = 7; g < 18; g++) { if (g == 12) wait_vm<0>(); bar(); }
    } else {
      int b = wid >> 1, wn = wid & 1;
      Frag<KC, 5> ft; ft.init(l0, 0, b * 5, lane);
      Frag<TR, 2> fw; fw.init(l0, WOFF, wn * 2, lane);
      f32x4 acc[2][5]; zero(acc);
      r_consumer<Ring<8192, 12>, Ring<YS, 3>>(fw, ft, acc);
      STAMP(0, 2);
      rsrc_t ro = mk_rsrc(ws + O_KC, 2 * SZ_TOK);
      int kb = pn * 2 + wn, sel = kb >= KD;
      unsigned oo = sel * SZ_TOK + (kb - sel * KD) * BLK, vo = ((q >> 1) * 16 + r) * 16 + (q & 1) * 8;
#pragma unroll
      for (int t = 0; t < 5; t++) {
        st64(pack4(acc[0][t]), ro, vo, oo + (b * 5 + t) * KD * BLK);
        st64(pack4(acc[1][t]), ro, vo + 512, oo + (b * 5 + t) * KD * BLK);
      }
      if (wid == 0) CODE_WARM(kf_main, KF_CODE, idx, l0 + K0_SCR);
      ENDSTAMP(0, 3);
    }
    return;
  }
  const int ax = o < 72 ? o >> 1 : o - 36;
  int pan = ax < 36 ? ax : ax + 14;
  GAS u64* flg = (GAS u64*)(ws + O_FLAG);
  u32x3 cv; u64 ep;
  { u32x4 dc = mk_desc(c, 2 * 2304); unsigned vo = (lane >> 5) * 2304 + wid * 384 + (lane & 31) * 12;
    asm volatile("buffer_load_dwordx3 %0, %1, %2, 0 offen" : "=v"(cv) : "v"(vo), "s"(dc) : "memory"); }
  asm volatile("global_load_dwordx2 %0, %1, %2 sc1" : "=v"(ep) : "v"(0u), "s"(flg) : "memory");
  auto wm_issue = [&]() {
    RSrc<8> s; s.tr(pan * 128 + wid * 192 * 13824, 13824, lane);
    u32x4 dw = mk_desc(wmod, DM * 13824);
#pragma unroll
    for (int g = 0; g < 3; g++) {
      dma4_asm(dw, l0 + wid * 24576 + g * 8192, s.v[0], s.v[1], s.v[2], s.v[3], g * 64 * 13824);
      dma4_asm(dw, l0 + wid * 24576 + g * 8192 + 4096, s.v[4], s.v[5], s.v[6], s.v[7], g * 64 * 13824);
    }
  };
  const int hb = ax - 14, hbat = hb >= 22, hj = (hb - 22 * hbat) * 3, ht = hbat * 64 + hj;
  const int hrows = hb < 0 ? 0 : hj == 63 ? 16 : 48;
  unsigned xo[18];
  auto x_addr = [&]() {
#pragma unroll
    for (int p = 0; p < 18; p++) {
      int ch = p * 64 + lane, row = (ch * 2731) >> 16, cc = ch - row * 24;
      xo[p] = row < hrows ? (ht * 16 + row) * 2304 + wid * 384 + ((cc ^ ((row >> 1) & 7)) << 4) - (p & 3) * 1024 + 4096 : OOB;
      pin(xo[p]);
    }
  };
  auto x_issue = [&]() {
    u32x4 d = mk_desc(x - 4096, 2048 * 2304 + 4096);
#pragma unroll
    for (int p = 0; p < 16; p += 4) dma4_asm(d, l0 + wid * 24576 + p * 1024, xo[p], xo[p + 1], xo[p + 2], xo[p + 3], 0);
    dma4_asm(d, l0 + wid * 24576 + 16384, xo[16], xo[17], OOB, OOB, 0);
  };
  LDS float* red = (LDS float*)(sm + 147456);
  LDS unsigned* sil = (LDS unsigned*)(sm + 147456 + 3072);
  LDS float* tabf = (LDS float*)(sm + 147456);
  LDS f32x2* st = (LDS f32x2*)(sm + 147456 + 9216);
  u32x4 cf[6];
  u64 tag;
  auto wm_mma = [&]() {
    Frag<TR, 4> fw; fw.init(l0, wid * 24576, 0, lane);
    f32x4 acc[1][4]; zero(acc); pin2(acc);
    sfor<0, 3>([&](auto i) {
      constexpr int g = decltype(i)::value;
      u32x4 w[2][4];
      sfor<0, 4>([&](auto jj) { fw.template read<0, decltype(jj)::value>(w[0][decltype(jj)::value], g * 8192); fw.template read<1, decltype(jj)::value>(w[1][decltype(jj)::value], g * 8192); });
      wait_lgkm<0>();
#pragma unroll
      for (int h = 0; h < 2; h++) {
        pin(cf[2 * g + h]);
#pragma unroll
        for (int j = 0; j < 4; j++) { pin(w[h][j]); mfma(acc[0][j], w[h][j], cf[2 * g + h]); }
      }
    });
    mfma_done(); pin2(acc);
    if (r < 2) {
#pragma unroll
      for (int j = 0; j < 4; j++) *(LDS f32x4*)(red + wid * 128 + r * 64 + j * 16 + q * 4) = acc[0][j];
    }
    wait_lgkm<0>(); bar();
    if (wid == 0) {
      f32x2 a = {0, 0};
#pragma unroll
      for (int w = 0; w < 6; w++) a += *(LDS f32x2*)(red + w * 128 + lane * 2);
      unsigned pv = pk2(a[0], a[1]);
      if (pan < 36) {
        unsigned tl = (unsigned)tag & 0xffff0000u, th = (unsigned)(tag >> 32);
        st128<SC1>(u32x4{tl | (pv & 0xffffu), th, tl | (pv >> 16), th}, mk_rsrc(ws + O_MG, 2 * 2304 * 8), (lane >> 5) * 18432 + pan * 512 + (lane & 31) * 16, 0);
      } else __builtin_amdgcn_raw_buffer_store_b32(pv, mk_rsrc(ws + O_MOD, 4608), (lane >> 5) * 2304 + (pan - 36) * 128 + (lane & 31) * 4, 0, 0);
    }
  };
  if (ax < 40) wm_issue();
  x_addr();
  if (ax >= 40) x_issue();
  asm volatile("s_waitcnt vmcnt(20)\n\ts_nop 0" : "+v"(cv), "+v"(ep)::"memory");
  tag = mix64(ep);
  if (ax < 40) {
#pragma unroll
    for (int j = 0; j < 3; j++) {
      float a = blo(cv[j]), b = bhi(cv[j]);
      sil[wid * 192 + lane * 3 + j] = pk2(a * __builtin_amdgcn_rcpf(1.f + __expf(-a)), b * __builtin_amdgcn_rcpf(1.f + __expf(-b)));
    }
    wait_lgkm<0>();
    unsigned sa = l0 + 147456 + 3072 + wid * 768 + (r & 1) * 384 + q * 16;
    sfor<0, 6>([&](auto i) { dsr128<decltype(i)::value * 64>(cf[decltype(i)::value], sa); });
    wait_vm<0>();
    STAMP(0, 7);
    wm_mma();
    if (wid) __builtin_amdgcn_s_sleep(3);
    if (hb >= 0) x_issue();
  }
  if (wid == 0 && lane == 0) __hip_atomic_store(flg + 8 + ax, tag, __ATOMIC_RELAXED, __HIP_MEMORY_SCOPE_AGENT);
  STAMP(0, 1);
  if (hb >= 0) {
    u32x4 ones = {0x3f803f80u, 0x3f803f80u, 0x3f803f80u, 0x3f803f80u}, id[2];
    { unsigned one = (r & 1) ? 0x3f800000u : 0x00003f80u; int w = (r >> 1) & 3;
#pragma unroll
      for (int t = 0; t < 2; t++)
#pragma unroll
        for (int j = 0; j < 4; j++) id[t][j] = (q == 2 * t + (r >> 3) && w == j) ? one : 0u; }
    pin(ones); pin(id[0]); pin(id[1]);
    f32x4 sa[3][2], xf[18][2]; zero(sa); pin2(sa);
    wait_vm<0>();
    STAMP(0, 5);
    u32x4 xv[3][6];
    { unsigned xp = l0 + wid * 24576 + r * 384, e = xp + ((q ^ ((r >> 1) & 7)) << 4), o = xp + (((4 + q) ^ ((r >> 1) & 7)) << 4);
      sfor<0, 18>([&](auto i) { constexpr int t = decltype(i)::value % 3, s = decltype(i)::value / 3; dsr128<t * 6144 + (s >> 1) * 128>(xv[t][s], s & 1 ? o : e); }); }
    wait_lgkm<0>();
#pragma unroll
    for (int s = 0; s < 6; s++)
#pragma unroll
      for (int t = 0; t < 3; t++) { mfma(sa[t][0], ones, xv[t][s]); mfma(sa[t][1], xv[t][s], xv[t][s]); }
#pragma unroll
    for (int i = 0; i < 18; i++) { mfma0(xf[i][0], id[0], xv[i / 6][i % 6]); mfma0(xf[i][1], id[1], xv[i / 6][i % 6]); }
    mfma_done(); pin2(sa);
    if (q == (r >> 2)) {
#pragma unroll
      for (int t = 0; t < 3; t++) { float d = (r & 2) ? ((r & 1) ? sa[t][1][3] : sa[t][1][2]) : ((r & 1) ? sa[t][1][1] : sa[t][1][0]); st[wid * 48 + t * 16 + r] = f32x2{sa[t][0][0], d}; }
    }
    wait_lgkm<0>(); bar();
    pin2(xf);
#pragma unroll
    for (int t = 0; t < 3; t++) {
      f32x2 a = {0, 0};
#pragma unroll
      for (int w = 0; w < 6; w++) a += st[w * 48 + t * 16 + r];
      float mean = a[0] * (1.f / DM), rstd = __builtin_amdgcn_rsqf(fmaxf(a[1] * (1.f / DM) - mean * mean, 0.f) + 1e-5f), nb = -mean * rstd;
#pragma unroll
      for (int s = 0; s < 6; s++) { xf[t * 6 + s][0] = xf[t * 6 + s][0] * rstd + nb; xf[t * 6 + s][1] = xf[t * 6 + s][1] * rstd + nb; }
    }
    pin2(xf);
    STAMP(0, 6);
    {
      rsrc_t rg = mk_rsrc(ws + O_MG, 2 * 2304 * 8);
      unsigned t = threadIdx.x, tl = (unsigned)tag & 0xffff0000u, th = (unsigned)(tag >> 32);
      u32x4 g[3];
#pragma unroll 1
      for (int spin = 0; spin < SPIN_CAP; spin++) {
        bool ok = true;
#pragma unroll
        for (int j = 0; j < 3; j++) {
          g[j] = ld128<SC1>(rg, t * 48 + j * 16, hbat * 18432);
          ok &= (g[j][0] & 0xffff0000u) == tl && g[j][1] == th && (g[j][2] & 0xffff0000u) == tl && g[j][3] == th;
        }
        if (__all(ok)) break;
        asm volatile("s_sleep 6" ::: "memory");
      }
      float one = t >= 192 ? 1.f : 0.f;
#pragma unroll
      for (int j = 0; j < 3; j++) *(LDS f32x2*)(tabf + t * 6 + j * 2) = f32x2{blo(g[j][0]) + one, blo(g[j][2]) + one};
    }
    STAMP(0, 2);
    wait_lgkm<0>(); bar();
    rsrc_t rh = mk_rsrc(ws + O_H, SZ_ACT);
    unsigned ho = (ht * KD + wid * 6) * BLK + lane * 16;
    LDS float* tp = tabf + wid * 192 + q * 4;
    u32x4 hv[18];
#pragma unroll
    for (int s = 0; s < 6; s++) {
      f32x4 f0 = *(LDS f32x4*)(tp + s * 32), f1 = *(LDS f32x4*)(tp + s * 32 + 16), a0 = *(LDS f32x4*)(tp + 1152 + s * 32), a1 = *(LDS f32x4*)(tp + 1152 + s * 32 + 16);
#pragma unroll
      for (int t = 0; t < 3; t++) hv[t * 6 + s] = pack8(xf[t * 6 + s][0] * a0 + f0, xf[t * 6 + s][1] * a1 + f1);
    }
    pin_all(hv);
#pragma unroll
    for (int i = 0; i < 18; i++) st128b<K0AUX>(hv[i], rh, ho, (i / 6) * 16 < hrows ? ((i / 6) * KD + i % 6) * BLK : OOB);
    st_end();
    STAMP(0, 4);
  } else {
    pan = 36 + ax;
    wait_lgkm<0>(); bar();
    wm_issue();
    wait_vm<0>();
    wm_mma();
  }
  { rsrc_t rz = mk_rsrc(ws + O_FL, CLR_BYTES);
    unsigned t = (ax * 384 + threadIdx.x) * 16;
    st128(u32x4{0, 0, 0, 0}, rz, t, 0); st128(u32x4{0, 0, 0, 0}, rz, t + 58 * 384 * 16, 0); }
  {
    unsigned v = lane * 16;
    if (wid == 0) CODE_WARM(kf_main, KF_CODE, idx, l0 + K0_SCR);
    if (WPF && (ax < 14 || ax >= 40)) {
      int first = ((ax < 14 ? ax : ax - 26) * 6 + wid) * WPF;
      u32x4 d1 = mk_desc(wqc, DM * 2304), d2 = mk_desc(woc, DM * 2304);
      for (int p = first; p < first + WPF; p += 4) {
        if (p < 2592) dma4_asm(d1, l0 + wid * 24576, v, v, v, v, p * 1024); else dma4_asm(d2, l0 + wid * 24576, v, v, v, v, (p - 2592) * 1024);
      }
    }
  }
  if (ax == 57 && wid == 0) {
#pragma unroll 1
    for (int spin = 0; spin < SPIN_CAP; spin++) {
      u64 f = __hip_atomic_load(flg + 8 + (lane < 58 ? lane : 0), __ATOMIC_RELAXED, __HIP_MEMORY_SCOPE_AGENT);
      if (__all(f == tag)) break;
      __builtin_amdgcn_s_sleep(4);
    }
    if (lane == 0) __hip_atomic_store(flg, tag, __ATOMIC_RELAXED, __HIP_MEMORY_SCOPE_AGENT);
  }
  ENDSTAMP(0, 3);
#endif
}

extern "C" void kernel_launch(void* const* d_in, const int* in_sizes, int n_in, void* d_out, int out_size, void* d_ws, size_t ws_size,
                              hipStream_t stream) {
  const char *x = (const char*)d_in[0], *c = (const char*)d_in[1], *y = (const char*)d_in[2], *wqkv = (const char*)d_in[3], *wo = (const char*)d_in[4],
             *wqc = (const char*)d_in[5], *wkvc = (const char*)d_in[6], *woc = (const char*)d_in[7], *wmod = (const char*)d_in[8];
  char* ws = (char*)d_ws;
  (void)hipFuncSetAttribute((const void*)k0_pre, hipFuncAttributeMaxDynamicSharedMemorySize, K0_LDS);
  (void)hipFuncSetAttribute((const void*)kf_main, hipFuncAttributeMaxDynamicSharedMemorySize, KF_LDS);
  k0_pre<<<256, 384, K0_LDS, stream>>>(x, c, y, wqkv, wo, wqc, wkvc, woc, wmod, ws);
  kf_main<<<256, 320, KF_LDS, stream>>>(x, wqc, woc, ws, (char*)d_out);
}
```
